```python
import torch

def _softmax(x, axis=-1):
    return torch.softmax(x, dim=axis)

def _gelu(x):
    return torch.nn.functional.gelu(x, approximate='tanh')

def _silu(x):
    return torch.nn.functional.silu(x)

def _sigmoid(x):
    return torch.sigmoid(x)

def _layernorm(x, axis=-1, eps=1e-05):
    m = x.mean(dim=axis, keepdim=True)
    v = x.var(dim=axis, keepdim=True, unbiased=False)
    return (x - m) / torch.sqrt(v + eps)

def _scatter_add(tgt, idx, src, axis=0):
    return tgt.clone().index_add_(axis, idx, src)

def _segment_sum(src, seg, n):
    out = torch.zeros((n,) + src.shape[1:], dtype=src.dtype)
    return out.index_add_(0, seg, src)

def _pad(x, pw):
    flat = tuple((v for pair in reversed(pw) for v in pair))
    return torch.nn.functional.pad(x, flat)
kernel_name = 'parallel_sum__memory_aug_recurrent_mem_tr__dense_activation_silu__prompt'

def setup_inputs():

    _w = lambda a: (lambda t: t.to(torch.bfloat16) if t.is_floating_point() else t)(torch.as_tensor(a))

    def _A_setup():
        B, S, Dm = (1, 2048, 512)
        H, Dh = (32, 128)
        M, seg = (8, 32)
        seg = min(seg, S)
        S = S // seg * seg
        return {'x': _w((lambda *s: torch.randn(*s, dtype=torch.bfloat16) * 0.05)(B, S, Dm)), 'mem0': _w((lambda *s: torch.randn(*s, dtype=torch.bfloat16) * 0.4)(B, M, Dm)), 'Wq': _w((lambda *s: torch.randn(*s, dtype=torch.bfloat16) * 3.2)(Dm, H, Dh) * Dm ** (-0.5)), 'Wk': _w((lambda *s: torch.randn(*s, dtype=torch.bfloat16) * 3.2)(Dm, H, Dh) * Dm ** (-0.5)), 'Wv': _w((lambda *s: torch.randn(*s, dtype=torch.bfloat16) * 0.4)(Dm, H, Dh) * Dm ** (-0.5)), 'Wo': _w((lambda *s: torch.randn(*s, dtype=torch.bfloat16) * 0.4)(H, Dh, Dm) * (H * Dh) ** (-0.5)), 'seg_len': seg}
    _A_in = _A_setup()

    def _B_setup():
        B, S, D, F = (1, 2048, 512, 1792)
        return {'x': _w((lambda *s: torch.randn(*s, dtype=torch.bfloat16) * 0.05)(B, S, D)), 'w1': _w((lambda *s: torch.randn(*s, dtype=torch.bfloat16) * 1.6)(D, F) * D ** (-0.5)), 'w2': _w((lambda *s: torch.randn(*s, dtype=torch.bfloat16) * 0.1)(F, D) * F ** (-0.5))}
    _B_in = _B_setup()
    return {'A_x': _A_in['x'], 'A_mem0': _A_in['mem0'], 'A_Wq': _A_in['Wq'], 'A_Wk': _A_in['Wk'], 'A_Wv': _A_in['Wv'], 'A_Wo': _A_in['Wo'], 'A_seg_len': _A_in['seg_len'], 'B_x': _B_in['x'], 'B_w1': _B_in['w1'], 'B_w2': _B_in['w2']}

def reference(A_x, A_mem0, A_Wq, A_Wk, A_Wv, A_Wo, A_seg_len, B_x, B_w1, B_w2):

    def _A_ref(x, mem0, Wq, Wk, Wv, Wo, seg_len):
        B, S, Dm = x.shape
        H, Dh = (Wq.shape[1], Wq.shape[2])
        M = mem0.shape[1]
        scale = Dh ** (-0.5)
        n_seg = S // seg_len
        L = M + seg_len
        idx = torch.arange(L)
        mask = (idx[:, None] < M) | (idx[:, None] >= idx[None, :])
        mem = mem0
        outs = []
        for s in range(n_seg):
            xs = x[:, s * seg_len:(s + 1) * seg_len, :].clone()
            h = torch.concatenate([mem, xs], axis=1)
            q = torch.einsum('btd,dhe->bhte', h, Wq)
            k = torch.einsum('btd,dhe->bhte', h, Wk)
            v = torch.einsum('btd,dhe->bhte', h, Wv)
            scores = torch.einsum('bhqe,bhke->bhqk', q, k) * scale
            scores = torch.where(mask[None, None], scores, -1000000000.0)
            p = _softmax(scores, axis=-1)
            o = torch.einsum('bhqk,bhke->bhqe', p, v)
            o = torch.einsum('bhte,hed->btd', o, Wo)
            mem = o[:, :M, :].clone()
            outs.append(o[:, M:, :])
        return torch.concatenate(outs, axis=1)

    def _B_ref(x, w1, w2):
        h = torch.einsum('bsd,df->bsf', x, w1)
        h = _silu(h)
        out = torch.einsum('bsf,fd->bsd', h, w2)
        return out
    x = A_x
    xn = _layernorm(x, axis=-1)
    return x + _A_ref(xn, A_mem0, A_Wq, A_Wk, A_Wv, A_Wo, A_seg_len) + _B_ref(xn, B_w1, B_w2)
```

```cpp
#include <hip/hip_runtime.h>
#include <hip/hip_bf16.h>
#include <stdint.h>

#define GAS __attribute__((address_space(1)))
#define LAS __attribute__((address_space(3)))
typedef unsigned v4u __attribute__((ext_vector_type(4)));
typedef unsigned v2u __attribute__((ext_vector_type(2)));
typedef unsigned v8u __attribute__((ext_vector_type(8)));
typedef __bf16 bf8 __attribute__((ext_vector_type(8)));
typedef __bf16 bf16v __attribute__((ext_vector_type(16)));
typedef __bf16 bf2 __attribute__((ext_vector_type(2)));
typedef float f4 __attribute__((ext_vector_type(4)));
typedef float f2 __attribute__((ext_vector_type(2)));
typedef unsigned short u16;
typedef __amdgpu_buffer_rsrc_t rsrc_t;

namespace {
constexpr int SEQ = 2048, DM = 512, NH = 32, HD = 128, NSEG = 64, FF = 1792;
constexpr unsigned SENT = 0xffffffffu, OOB = 0x7fffff00u;
constexpr int BLK = 32768;
constexpr size_t O_COMM = 0;
constexpr size_t O_XN = 512 << 10;
constexpr size_t O_BS = O_XN + (2u << 20);
constexpr size_t O_KX = O_BS + (16u << 20);
constexpr size_t O_VO = O_KX + (16u << 20);
constexpr size_t O_UM = O_VO + (64u << 20);
constexpr size_t O_KM = O_UM + (16u << 20);
constexpr size_t O_PX = O_KM + (4u << 20);
constexpr size_t O_PM = O_PX + (4u << 20);
constexpr size_t O_HH = O_PM + (1u << 20);
constexpr size_t O_END = O_HH + (7u << 20);
constexpr unsigned C_MET = 12 * BLK;
constexpr unsigned C_MSG0 = 13 * BLK;
constexpr unsigned C_KXF = 14 * BLK, C_VOF = C_KXF + 8192, C_KMF = C_VOF + 8192, C_UMF = C_KMF + 4096, C_CFG = C_UMF + 8192;
constexpr unsigned C_PTF = 15 * BLK, C_HF = C_PTF + 8192, C_CLAIM = C_HF + 4096;
constexpr int NREP = 4;
#define T_PERIOD 3600
#define T_SEND 1000
#define T_DA 20
#define T_DB 102
#define T_GUARD 1000
#define T_DU 24
constexpr int SPIN = 1 << 17;

struct Args {
    const u16* x; const u16* mem0; const u16* wq; const u16* wk; const u16* wv; const u16* wo; const u16* w1; const u16* w2;
    u16* out; unsigned char* ws;
};

__device__ __forceinline__ rsrc_t mkrsrc(const void* p, unsigned bytes) { return __builtin_amdgcn_make_buffer_rsrc((void*)p, 0, bytes, 0x00020000); }
__device__ __forceinline__ unsigned ck() { return (unsigned)__builtin_amdgcn_s_memtime(); }
__device__ __forceinline__ unsigned xcc_id() { return (unsigned)__builtin_amdgcn_s_getreg(20 | (3 << 11)) & 0xfu; }
__device__ __forceinline__ unsigned pk(float lo, float hi) { f2 v = {lo, hi}; bf2 r = __builtin_convertvector(v, bf2); return __builtin_bit_cast(unsigned, r); }
__device__ __forceinline__ float bflo(unsigned w) { return __builtin_bit_cast(float, w << 16); }
__device__ __forceinline__ float bfhi(unsigned w) { return __builtin_bit_cast(float, w & 0xffff0000u); }
template <int CTRL> __device__ __forceinline__ float dppf(float v) { return __builtin_bit_cast(float, __builtin_amdgcn_update_dpp(0, __builtin_bit_cast(int, v), CTRL, 0xf, 0xf, true)); }
__device__ __forceinline__ bf16v cat(v4u a, v4u b) { v8u r = {a.x, a.y, a.z, a.w, b.x, b.y, b.z, b.w}; return __builtin_bit_cast(bf16v, r); }
__device__ __forceinline__ f4 mfma(v4u a, v4u b, f4 c) { return __builtin_amdgcn_mfma_f32_16x16x32_bf16(__builtin_bit_cast(bf8, a), __builtin_bit_cast(bf8, b), c, 0, 0, 0); }
__device__ __forceinline__ f4 smfma(v4u a, v4u b0, v4u b1, f4 c, int idx) { return __builtin_amdgcn_smfmac_f32_16x16x64_bf16(__builtin_bit_cast(bf8, a), cat(b0, b1), c, idx, 0, 0); }
__device__ __forceinline__ v4u ld128(rsrc_t r, unsigned v, unsigned s) { return __builtin_amdgcn_raw_buffer_load_b128(r, v, s, 0); }
__device__ __forceinline__ v4u ld128c(rsrc_t r, unsigned v, unsigned s) { return __builtin_amdgcn_raw_buffer_load_b128(r, v, s, 16); }
__device__ __forceinline__ void st128c(v4u d, rsrc_t r, unsigned v, unsigned s) { __builtin_amdgcn_raw_buffer_store_b128(d, r, v, s, 16); }
__device__ __forceinline__ void st64c(v2u d, rsrc_t r, unsigned v, unsigned s) { __builtin_amdgcn_raw_buffer_store_b64(d, r, v, s, 16); }
__device__ __forceinline__ void st32c(unsigned d, rsrc_t r, unsigned v, unsigned s) { __builtin_amdgcn_raw_buffer_store_b32(d, r, v, s, 16); }
#define BAR() asm volatile("s_waitcnt lgkmcnt(0)\n\ts_barrier" ::: "memory")
#define DRAIN() asm volatile("s_waitcnt vmcnt(0)" ::: "memory")
#define FENCE() asm volatile("" ::: "memory")

__device__ __forceinline__ void sled(int n) {
    const int m = __builtin_amdgcn_readfirstlane(min(max(n, 0), 256));
    asm volatile("s_getpc_b64 vcc\n\ts_add_u32 vcc_lo, vcc_lo, %0\n\ts_addc_u32 vcc_hi, vcc_hi, 0\n\ts_setpc_b64 vcc\n\t.rept 256\n\ts_nop 3\n\t.endr" :: "s"(12 + (256 - m) * 4) : "scc", "vcc", "memory");
}
template <int N> __device__ __forceinline__ void pause16() { asm volatile(".rept %0\n\ts_nop 3\n\t.endr" :: "i"(N) : "memory"); }

struct PollRegs { unsigned a, b, c, d, e, f; };
#define PSTEP(r) "s_waitcnt vmcnt(5)\n\tv_cmp_le_u32 vcc, %[want], " r "\n\tglobal_load_dword " r ", %[addr], off sc1\n\ts_cbranch_vccnz 2f\n\t"
__device__ __forceinline__ int poll_dense(const GAS unsigned* p, unsigned want, PollRegs& pr) {
    int cnt = SPIN;
    asm volatile(
        "s_waitcnt vmcnt(0)\n\t"
        "global_load_dword %0, %[addr], off sc1\n\ts_nop 7\n\t"
        "global_load_dword %1, %[addr], off sc1\n\ts_nop 7\n\t"
        "global_load_dword %2, %[addr], off sc1\n\ts_nop 7\n\t"
        "global_load_dword %3, %[addr], off sc1\n\ts_nop 7\n\t"
        "global_load_dword %4, %[addr], off sc1\n\ts_nop 7\n\t"
        "global_load_dword %5, %[addr], off sc1\n\t"
        "1:\n\t"
        PSTEP("%0") PSTEP("%1") PSTEP("%2") PSTEP("%3") PSTEP("%4") PSTEP("%5")
        "s_sub_u32 %[cnt], %[cnt], 1\n\ts_cmp_lg_u32 %[cnt], 0\n\ts_cbranch_scc1 1b\n\t"
        "2:\n\t"
        : "=&v"(pr.a), "=&v"(pr.b), "=&v"(pr.c), "=&v"(pr.d), "=&v"(pr.e), "=&v"(pr.f), [cnt] "+s"(cnt)
        : [addr] "v"(p), [want] "s"(want) : "vcc", "scc", "memory");
    return cnt;
}
__device__ __forceinline__ void poll_drain(PollRegs& pr) {
    asm volatile("s_waitcnt vmcnt(0)" : "+v"(pr.a), "+v"(pr.b), "+v"(pr.c), "+v"(pr.d), "+v"(pr.e), "+v"(pr.f) :: "memory");
}
__device__ __forceinline__ bool wait_dense(const GAS unsigned* p, unsigned want) { PollRegs pr; const int c = poll_dense(p, want, pr); poll_drain(pr); return c != 0; }
__device__ __forceinline__ bool wait_flags(rsrc_t rc, unsigned voff, unsigned want) {
    for (int i = 0; i < SPIN * 4; ++i) {
        FENCE();
        const unsigned v = __builtin_amdgcn_raw_buffer_load_b32(rc, voff, 0, 16);
        if (__builtin_amdgcn_ballot_w64(v < want) == 0) { FENCE(); return true; }
        __builtin_amdgcn_s_sleep(2);
    }
    return false;
}

template <int KS, class RowF>
__device__ __forceinline__ void load_w4(rsrc_t r, unsigned voff, unsigned rowbytes, RowF rowf, v4u (&W)[KS][4]) {
#pragma unroll
    for (int ks = 0; ks < KS; ++ks) {
        v2u R[8];
#pragma unroll
        for (int jj = 0; jj < 8; ++jj) R[jj] = __builtin_amdgcn_raw_buffer_load_b64(r, voff, rowf(ks, jj) * rowbytes, 0);
#pragma unroll
        for (int t = 0; t < 4; ++t)
#pragma unroll
            for (int q = 0; q < 4; ++q) W[ks][t][q] = __builtin_amdgcn_perm(R[2 * q + 1][t >> 1], R[2 * q][t >> 1], (t & 1) ? 0x07060302u : 0x05040100u);
    }
}
template <int KS, class RowF>
__device__ __forceinline__ void load_w8(rsrc_t r, unsigned voff, unsigned rowbytes, RowF rowf, v4u (&W)[KS][8]) {
#pragma unroll
    for (int ks = 0; ks < KS; ++ks) {
        v4u R[8];
#pragma unroll
        for (int jj = 0; jj < 8; ++jj) R[jj] = ld128(r, voff, rowf(ks, jj) * rowbytes);
#pragma unroll
        for (int t = 0; t < 8; ++t)
#pragma unroll
            for (int q = 0; q < 4; ++q) W[ks][t][q] = __builtin_amdgcn_perm(R[2 * q + 1][t >> 1], R[2 * q][t >> 1], (t & 1) ? 0x07060302u : 0x05040100u);
    }
}

__global__ void __launch_bounds__(256, 2) prep_kernel(Args a) {
    const int b = blockIdx.x, tid = threadIdx.x, wave = __builtin_amdgcn_readfirstlane(tid >> 6), lane = tid & 63, g = lane >> 4, n = lane & 15;
    if (b >= 256) {
        const int j = b - 256;
        {
            const unsigned off = (unsigned)(j * 256 + tid) * 8u;
            const unsigned blk = off / BLK;
            v2u v = blk < 12 ? (v2u){SENT, SENT} : (v2u){0u, 0u};
            if (blk == 13 && (off & (BLK - 1)) < 8192) {
                const unsigned o = off & 8191, f = o >> 10, G = (o >> 8) & 3, ih = (o >> 7) & 1, p = (o >> 6) & 1, i3 = (o >> 4) & 3, ah = (o >> 3) & 1;
                const u16* src = a.mem0 + (4 * ih + i3) * DM + 64 * f + 2 * G + p + 32 * ah;
                v.x = min((unsigned)src[0] | ((unsigned)src[8] << 16), SENT - 1); v.y = min((unsigned)src[16] | ((unsigned)src[24] << 16), SENT - 1);
            }
            *(GAS v2u*)(a.ws + O_COMM + off) = v;
        }
#pragma unroll
        for (int k = 0; k < 2; ++k) {
            const int tok = j * 8 + wave * 2 + k;
            const v4u v = *(const GAS v4u*)(a.x + (size_t)tok * DM + lane * 8);
            float e[8] = {bflo(v.x), bfhi(v.x), bflo(v.y), bfhi(v.y), bflo(v.z), bfhi(v.z), bflo(v.w), bfhi(v.w)};
            float s = 0.f;
#pragma unroll
            for (int q = 0; q < 8; ++q) s += e[q];
#pragma unroll
            for (int m = 32; m >= 1; m >>= 1) s += __shfl_xor(s, m);
            const float mean = s * (1.f / DM);
            float ss = 0.f;
#pragma unroll
            for (int q = 0; q < 8; ++q) { e[q] -= mean; ss += e[q] * e[q]; }
#pragma unroll
            for (int m = 32; m >= 1; m >>= 1) ss += __shfl_xor(ss, m);
            const float rs = 1.f / sqrtf(ss * (1.f / DM) + 1e-5f);
            const v4u o = {pk(e[0] * rs, e[1] * rs), pk(e[2] * rs, e[3] * rs), pk(e[4] * rs, e[5] * rs), pk(e[6] * rs, e[7] * rs)};
            *(GAS v4u*)(a.ws + O_XN + (size_t)(tok >> 4) * 16384 + (lane >> 2) * 1024 + ((lane & 3) * 16 + (tok & 15)) * 16) = o;
        }
        return;
    }
    const int h = (b & 7) * 4 + ((b >> 3) & 3), f = b >> 5;
    const rsrc_t rv = mkrsrc(a.wv, DM * 4096 * 2), ro = mkrsrc(a.wo, 4096 * DM * 2);
    v4u A[4][4];
    {
        const int gg = n >> 2, c = n & 3;
#pragma unroll
        for (int dt = 0; dt < 4; ++dt) {
            const int hi = dt >> 1, half = dt & 1;
            const int d = 64 * f + 8 * (4 * (gg & 1) + 2 * half + (c & 1)) + 2 * (2 * hi + (gg >> 1)) + (c >> 1);
#pragma unroll
            for (int ks = 0; ks < 4; ++ks) A[dt][ks] = ld128(rv, (unsigned)(d * 4096 + h * HD + 8 * g) * 2, ks * 64);
        }
    }
    v4u W[4][8];
    load_w8<4>(ro, (unsigned)((h * HD + 8 * g) * DM + 128 * wave + 8 * n) * 2, DM * 2, [](int ks, int jj) { return 32 * ks + jj; }, W);
    const rsrc_t rb = mkrsrc(a.ws + O_BS, 16u << 20);
    const unsigned vo = (unsigned)(16 * wave + 8 * (n >> 3)) * 262144u + (h >> 3) * 65536u + f * 8192u + ((h >> 1) & 3) * 2048u + (g * 16 + (h & 1) * 8 + (n & 7)) * 16u;
#pragma unroll
    for (int t = 0; t < 8; ++t)
#pragma unroll
        for (int hi = 0; hi < 2; ++hi) {
            f4 d0 = {0, 0, 0, 0}, d1 = d0;
#pragma unroll
            for (int ks = 0; ks < 4; ++ks) { d0 = mfma(A[2 * hi][ks], W[ks][t], d0); d1 = mfma(A[2 * hi + 1][ks], W[ks][t], d1); }
            const v4u o = {pk(d0[0], d0[1]), pk(d0[2], d0[3]), pk(d1[0], d1[1]), pk(d1[2], d1[3])};
            __builtin_amdgcn_raw_buffer_store_b128(o, rb, vo + hi * 1024, t * 262144u, 0);
        }
}

template <int ROT, int SP>
__device__ __forceinline__ void mem_load(rsrc_t r, unsigned voff, unsigned so0, unsigned so1, v4u (&m)[8]) {
    FENCE();
#pragma unroll
    for (int k = 0; k < 8; ++k) {
        const int f = (k + ROT) & 7;
        m[f] = ld128c(r, voff + (f & 3) * 1024, f < 4 ? so0 : so1);
        if (SP && k < 7) { __builtin_amdgcn_sched_barrier(0); asm volatile("s_nop %0" :: "i"(SP) : "memory"); __builtin_amdgcn_sched_barrier(0); }
    }
}
template <int ROT>
__device__ __forceinline__ void mem_mma(const v4u (&m)[8], const v4u (&W)[16][4], int idx, f4 (&acc)[4]) {
#pragma unroll
    for (int t = 0; t < 4; ++t) { acc[t] = (f4){0.f, 0.f, 0.f, 0.f}; asm volatile("" : "+v"(acc[t])); }
#pragma unroll
    for (int k = 0; k < 8; ++k) {
        const int f = (k + ROT) & 7;
#pragma unroll
        for (int t = 0; t < 4; ++t) acc[t] = smfma(m[f], W[2 * f][t], W[2 * f + 1][t], acc[t], idx);
    }
#pragma unroll
    for (int t = 0; t < 4; ++t) { acc[t][0] += acc[t][2]; acc[t][1] += acc[t][3]; }
}
template <int N> __device__ __forceinline__ bool has_sent(const v4u (&m)[N]) {
    unsigned x = 0;
#pragma unroll
    for (int f = 0; f < N; ++f) x = max(x, max(max(m[f].x, m[f].y), max(m[f].z, m[f].w)));
    return __builtin_amdgcn_ballot_w64(x == SENT) != 0;
}
__device__ __forceinline__ unsigned page_of(unsigned nb, int k) { return (nb * 2 + (k & 1) + (k >> 1) * 4) * 4096u; }

template <int wave>
__device__ __forceinline__ void head_wave(const Args& a, LAS unsigned char* L, int head, int lane, int rep) {
    const int g = lane >> 4, n = lane & 15;
    const rsrc_t rc = mkrsrc(a.ws + O_COMM, 16 * BLK);
    v4u W[16][4];
    load_w4<16>(mkrsrc(wave < 2 ? a.wq : a.wk, DM * 4096 * 2), (unsigned)((32 * (g & 1) + 2 * (g >> 1)) * 4096 + head * HD + 64 * (wave & 1) + 4 * n) * 2, 8192,
                [](int ks, int jj) { return 64 * (ks >> 1) + 4 * (ks & 1) + 16 * (jj >> 2) + 8 * (jj & 1) + ((jj >> 1) & 1); }, W);
    const int p = (n >> 1) & 1, i = 2 * (n >> 2) + (n & 1);
    const int idx = p ? 0xEEEE : 0x4444;
    const unsigned mvoff = g * 256 + (i >> 2) * 128 + p * 64 + (i & 3) * 16;
    constexpr int QS = 288;
    if (!wait_flags(rc, C_CFG, 1)) return;
    const unsigned nb = __builtin_amdgcn_readfirstlane(__builtin_amdgcn_raw_buffer_load_b32(rc, C_CFG, 0, 16)) - 1;
    const unsigned pm0 = page_of(nb, 0), pm1 = page_of(nb, 1), pp0 = page_of(nb, 2), pp1 = page_of(nb, 3);
    const GAS unsigned* flag_rd = (const GAS unsigned*)(a.ws + O_COMM + C_MET + pp0 + rep * 128);
    const rsrc_t rk = mkrsrc(a.ws + O_KX, 16u << 20), rkm = mkrsrc(a.ws + O_KM, 4u << 20);
    v4u kx[8];
    auto kx_load = [&](int sn) {
#pragma unroll
        for (int q = 0; q < 8; ++q) kx[q] = ld128c(rk, lane * 16 + (q & 3) * 1024, (sn * NH + head) * 8192 + (q >> 2) * 4096);
    };
    if (wave < 2) kx_load(0);
    const float csc = 0.08838834764831845f * 1.4426950408889634f;
    const unsigned px_so = (head >> 4) * BLK + (((head >> 3) & 1) ? pp1 : pp0), pm_so = 2 * BLK + pp0;
    LAS unsigned* wr = (LAS unsigned*)(L + (wave < 2 ? 0 : 8 * QS) + 2 * g * QS + 128 * (wave & 1) +
                                       (wave < 2 ? 32 * (n >> 3) + 16 * (n & 1) + 8 * ((n >> 2) & 1) + 4 * ((n >> 1) & 1) : 4 * n));
    const unsigned pxv = px_so + (head & 7) * 512 + wave * 256 + (n >> 2) * 64 + g * 16 + (n & 3) * 4;
    const unsigned pmv = (n < 8 && !(n & 1)) ? pm_so + head * 128 + wave * 64 + g * 16 + n * 2 : OOB;
    const unsigned clv = lane < 32 ? px_so + (head & 7) * 512 + lane * 16 : lane < 40 ? pm_so + head * 128 + (lane - 32) * 16 : OOB;
    PollRegs pr;
    for (int s = 0; s < NSEG; ++s) {
        const int slot = s & 3;
        const unsigned so0 = s ? slot * BLK + pm0 : C_MSG0, so1 = s ? slot * BLK + pm1 : C_MSG0 + 4096;
        f4 acc[4]; v4u am[8];
        bool fail = false;
        for (int att = 0;; ++att) {
            mem_load<2 * wave, 8>(rc, mvoff, so0, so1, am);
            mem_mma<2 * wave>(am, W, idx, acc);
            const unsigned long long bad = __builtin_amdgcn_ballot_w64(__builtin_isunordered(acc[0][0], acc[0][1]));
            wr[0] = pk(acc[0][0], acc[1][0]); wr[16] = pk(acc[2][0], acc[3][0]); wr[QS / 4] = pk(acc[0][1], acc[1][1]); wr[QS / 4 + 16] = pk(acc[2][1], acc[3][1]);
            if (__builtin_expect(bad == 0, 1)) break;
            if (!has_sent<8>(am)) break;
            if (att == 0 && !wait_dense(flag_rd, 2 * s + 1)) { fail = true; break; }
            if (att > SPIN) { fail = true; break; }
        }
        BAR();
        if (wave < 2) {
            if (s < NSEG - 1) {
                const LAS unsigned char* qa = L + i * QS + 32 * g + 16 * p;
                const v4u q0 = *(const LAS v4u*)qa, q1 = *(const LAS v4u*)(qa + 128);
                const LAS unsigned char* ka = L + 8 * QS + (n & 7) * QS + 16 * g;
                const v4u k00 = *(const LAS v4u*)ka, k01 = *(const LAS v4u*)(ka + 64), k10 = *(const LAS v4u*)(ka + 128), k11 = *(const LAS v4u*)(ka + 192);
                f4 s0 = {0, 0, 0, 0}, s1 = s0, sm = s0;
                asm volatile("" : "+v"(s0), "+v"(s1), "+v"(sm));
                s0 = smfma(q0, kx[0], kx[1], s0, idx); s1 = smfma(q0, kx[4], kx[5], s1, idx);
                s0 = smfma(q1, kx[2], kx[3], s0, idx); s1 = smfma(q1, kx[6], kx[7], s1, idx);
                sm = smfma(q0, k00, k01, sm, idx); sm = smfma(q1, k10, k11, sm, idx);
                constexpr int c = wave & 1;
                const float a0 = s0[c] + s0[c + 2], a1 = s1[c] + s1[c + 2], amm = sm[c] + sm[c + 2];
                float mx = fmaxf(fmaxf(a0, a1), amm);
                mx = fmaxf(mx, dppf<0x128>(mx)); mx = fmaxf(mx, dppf<0x124>(mx)); mx = fmaxf(mx, dppf<0x122>(mx)); mx = fmaxf(mx, dppf<0x121>(mx));
                const float mc = mx * csc;
                const float e0 = __builtin_amdgcn_exp2f(__builtin_fmaf(a0, csc, -mc)), e1 = __builtin_amdgcn_exp2f(__builtin_fmaf(a1, csc, -mc)), em = __builtin_amdgcn_exp2f(__builtin_fmaf(amm, csc, -mc));
                float sum = __builtin_fmaf(em, 0.5f, e0 + e1);
                sum += dppf<0x128>(sum); sum += dppf<0x124>(sum); sum += dppf<0x122>(sum); sum += dppf<0x121>(sum);
                const float rs = __builtin_amdgcn_rcpf(sum);
                const unsigned pso = 3 * slot * BLK;
                st32c(pk(e0 * rs, e1 * rs) & 0x7fff7fffu, rc, pxv, pso);
                st32c(pk(em * rs, dppf<0xB1>(em) * rs) & 0x7fff7fffu, rc, pmv, pso);
                pause16<4>();
                if (wave == 1) st128c((v4u){SENT, SENT, SENT, SENT}, rc, clv, 3 * ((s + 2) & 3) * BLK);
                if (s < NSEG - 2) kx_load(s + 1);
            }
        } else if (wave == 2) {
            if (s < NSEG - 1) {
                if (!poll_dense(flag_rd, 2 * s + 2, pr)) fail = true;
                pause16<T_DB>();
                poll_drain(pr);
            }
        } else {
            const LAS unsigned char* ks = L + 8 * QS + g * QS + n * 16;
            const v4u k0 = *(const LAS v4u*)ks, k1 = *(const LAS v4u*)(ks + 4 * QS);
            if (s < NSEG - 1) pause16<36>();
            st128c(k0, rkm, lane * 16, (s * NH + head) * 2048); st128c(k1, rkm, lane * 16 + 1024, (s * NH + head) * 2048);
            DRAIN();
            st32c(s + 1, rc, lane == 0 ? C_KMF + head * 128 : OOB, 0);
        }
        if (fail) break;
        BAR();
    }
    DRAIN();
}

template <int wave>
__device__ __forceinline__ void slice_wave(const Args& a, LAS unsigned char* L, int sl, int lane, int rep) {
    const int g = lane >> 4, n = lane & 15;
    const rsrc_t rc = mkrsrc(a.ws + O_COMM, 16 * BLK);
    v4u W[16][4];
    {
        const rsrc_t rb = mkrsrc(a.ws + O_BS + (size_t)sl * 262144 + wave * 65536, 65536);
#pragma unroll
        for (int f = 0; f < 8; ++f)
#pragma unroll
            for (int t = 0; t < 4; ++t) { W[2 * f][t] = ld128(rb, lane * 16 + (t & 1) * 2048, f * 8192 + (t >> 1) * 4096); W[2 * f + 1][t] = ld128(rb, lane * 16 + (t & 1) * 2048 + 1024, f * 8192 + (t >> 1) * 4096); }
    }
    const int p = (n >> 1) & 1, i = 2 * (n >> 2) + (n & 1);
    const int idx = p ? 0xEEEE : 0x4444;
    const unsigned mvoff = g * 256 + (i >> 2) * 128 + p * 64 + (i & 3) * 16;
    constexpr int UXO = 8192;
    if (!wait_flags(rc, C_CFG, 1)) return;
    const unsigned nb = __builtin_amdgcn_readfirstlane(__builtin_amdgcn_raw_buffer_load_b32(rc, C_CFG, 0, 16)) - 1;
    const unsigned pm0 = page_of(nb, 0), pm1 = page_of(nb, 1), pp0 = page_of(nb, 2), pp1 = page_of(nb, 3);
    const GAS unsigned* flag_rd = (const GAS unsigned*)(a.ws + O_COMM + C_MET + pp0 + rep * 128);
    const rsrc_t rv = mkrsrc(a.ws + O_VO, 64u << 20), rum = mkrsrc(a.ws + O_UM, 16u << 20);
    const unsigned uvoff = p * 512 + g * 128 + i * 16;
    v4u ux[16];
    auto ux_load = [&](int sn) {
#pragma unroll
        for (int F = 0; F < 16; ++F) ux[F] = ld128c(rv, lane * 16 + (F & 3) * 1024, (sn * 64 + sl) * 16384 + (F >> 2) * 4096);
    };
    if (wave == 3) ux_load(0);
    const int pblk = n >> 3, ppar = (n >> 2) & 1, pg = n & 3, orow = 2 * pg + ppar;
    const unsigned pxoff = pblk * 512 + ppar * 256 + g * 64 + pg * 16, pmoff = (4 * pblk + g) * 128 + ppar * 64 + pg * 16;
    const unsigned mbase = ((sl & 31) >> 1) * 256 + (sl & 1) * 64 + ((sl >> 5) ? pm1 : pm0);
    const unsigned mst = n < 8 ? mbase + (orow >> 2) * 128 + (orow & 3) * 16 + g * 4 : OOB;
    const unsigned clv = lane < 8 ? mbase + (lane >> 2) * 128 + (lane & 3) * 16 : OOB;
    PollRegs pr;
    for (int s = 0; s < NSEG; ++s) {
        const int slot = s & 3;
        const unsigned so0 = s ? slot * BLK + pm0 : C_MSG0, so1 = s ? slot * BLK + pm1 : C_MSG0 + 4096;
        f4 acc[4]; v4u am[8];
        bool fail = false;
        const bool last = s == NSEG - 1;
        for (int att = 0;; ++att) {
            mem_load<2 * wave, 8>(rc, mvoff, so0, so1, am);
            mem_mma<2 * wave>(am, W, idx, acc);
            const unsigned long long bad = __builtin_amdgcn_ballot_w64(__builtin_isunordered(acc[0][0], acc[0][1]));
#pragma unroll
            for (int t = 0; t < 4; ++t) *(LAS unsigned*)(L + (8 * wave + 2 * t) * 128 + n * 16 + g * 4) = pk(acc[t][0], acc[t][1]);
            if (__builtin_expect(bad == 0, 1)) break;
            if (!has_sent<8>(am)) break;
            if (att == 0 && !wait_dense(flag_rd, 2 * s + 1)) { fail = true; break; }
            if (att > SPIN) { fail = true; break; }
        }
        if (wave == 3 && !last) {
#pragma unroll
            for (int F = 0; F < 16; ++F) *(LAS v4u*)(L + UXO + F * 1024 + lane * 16) = ux[F];
        }
        if (wave == 0) {
            const unsigned pso = 3 * slot * BLK;
            unsigned q0 = pso + pp0, q1 = pso + pp1, q2 = q0 + BLK, q3 = q1 + BLK, q4 = q0 + 2 * BLK, nso = ((s + 1) & 3) * BLK;
            asm volatile("" : "+s"(q0), "+s"(q1), "+s"(q2), "+s"(q3), "+s"(q4), "+s"(nso));
            BAR();
            if (!last) {
                unsigned nanw = SENT;
                for (int att = 0;; ++att) {
                    v4u pf[20], um[4];
                    FENCE();
#pragma unroll
                    for (int F = 0; F < 16; ++F) pf[F] = ld128c(rc, pxoff + (F & 3) * 1024, (F >> 2) == 0 ? q0 : (F >> 2) == 1 ? q1 : (F >> 2) == 2 ? q2 : q3);
#pragma unroll
                    for (int F = 0; F < 4; ++F) pf[16 + F] = ld128c(rc, pmoff + F * 1024, q4);
                    __builtin_amdgcn_sched_barrier(0);
#pragma unroll
                    for (int F = 0; F < 16; ++F) ux[F] = *(const LAS v4u*)(L + UXO + F * 1024 + uvoff);
#pragma unroll
                    for (int F = 0; F < 4; ++F) um[F] = *(const LAS v4u*)(L + (8 * F + 4 * p + g) * 128 + i * 16);
                    __builtin_amdgcn_sched_barrier(0);
                    f4 d = {0, 0, 0, 0};
#pragma unroll
                    for (int F = 0; F < 16; ++F) d = mfma(ux[F], pf[F], d);
#pragma unroll
                    for (int F = 0; F < 4; ++F) d = mfma(um[F], pf[16 + F], d);
                    const float r0 = d[0] + dppf<0x128>(d[2]), r1 = d[1] + dppf<0x128>(d[3]);
                    const bool un = __builtin_isunordered(r0, r1);
                    st32c(un ? nanw : pk(r0, r1), rc, mst, nso);
                    if (__builtin_expect((__builtin_amdgcn_ballot_w64(un) & 0x00ff00ff00ff00ffull) == 0, 1) || nanw != SENT) break;
                    if (!has_sent<20>(pf)) nanw = 0x7fc07fc0u;
                    if (att > SPIN) { fail = true; break; }
                }
            }
        } else if (wave == 1) {
            if (!last) {
                if (!poll_dense(flag_rd, 2 * s + 2, pr)) fail = true;
                pause16<T_DA>();
                BAR();
                pause16<T_DB + 2 - T_DA>();
                poll_drain(pr);
            } else BAR();
        } else if (wave == 2) {
            BAR();
            v4u u[4];
#pragma unroll
            for (int q = 0; q < 4; ++q) u[q] = *(const LAS v4u*)(L + q * 1024 + lane * 16);
            if (!last) { pause16<T_DU>(); st128c((v4u){SENT, SENT, SENT, SENT}, rc, clv, ((s + 3) & 3) * BLK); }
#pragma unroll
            for (int q = 0; q < 4; ++q) st128c(u[q], rum, lane * 16 + q * 1024, (s * 64 + sl) * 4096);
            DRAIN();
            st32c(s + 1, rc, lane == 0 ? C_UMF + sl * 128 : OOB, 0);
        } else {
            BAR();
            if (s < NSEG - 2) { pause16<T_DU + 6>(); ux_load(s + 1); }
        }
        if (fail) break;
        BAR();
    }
    DRAIN();
}

__device__ __forceinline__ void metronome(const Args& a, int lane) {
    const rsrc_t rc = mkrsrc(a.ws + O_COMM, 16 * BLK);
    unsigned best[2] = {0xffffffffu, 0xffffffffu};
    for (int r = 0; r < 12; ++r) {
#pragma unroll
        for (int k = 0; k < 2; ++k) {
            const GAS unsigned* q = (const GAS unsigned*)(a.ws + O_COMM + C_MSG0 + (4 + 2 * k + (r & 1)) * 4096 + r * 256);
            unsigned v = r;
            asm volatile("global_store_dword %0, %1, off sc1\n\ts_waitcnt vmcnt(0)" :: "v"(q), "v"(v) : "memory");
            const unsigned c1 = ck();
            asm volatile("s_waitcnt lgkmcnt(0)\n\tglobal_load_dword %0, %1, off sc1\n\ts_waitcnt vmcnt(0)" : "=v"(v) : "v"(q) : "memory");
            const unsigned c2 = ck();
            best[k] = min(best[k], c2 - c1);
        }
    }
    const unsigned nb = best[1] < best[0] ? 1u : 0u;
    const unsigned pm0 = page_of(nb, 0), pm1 = page_of(nb, 1), pp0 = page_of(nb, 2);
    const unsigned fst = lane < 4 * NREP ? C_MET + pp0 + lane * 128 : OOB;
    auto need = [&](int s) -> unsigned { if (s > NSEG - 2) s = NSEG - 2; return (unsigned)((s + 2 - (lane & 1)) >> 1); };
    if (!wait_flags(rc, C_KXF + lane * 128, need(1)) || !wait_flags(rc, C_VOF + lane * 128, need(0))) return;
    st32c(nb + 1, rc, lane == 0 ? C_CFG : OOB, 0);
    unsigned tr = ck() + T_GUARD;
    for (int s = 0; s < NSEG; ++s) {
        const int slot = s & 3;
        const unsigned so0 = s ? slot * BLK + pm0 : C_MSG0, so1 = s ? slot * BLK + pm1 : C_MSG0 + 4096;
        if (!wait_flags(rc, C_KXF + lane * 128, need(s + 2)) || !wait_flags(rc, C_VOF + lane * 128, need(s + 1))) return;
        v4u am[8];
        sled(((int)(tr - ck())) >> 4);
        mem_load<0, 0>(rc, lane * 16, so0, so1, am);
        if (__builtin_expect(has_sent<8>(am), 0)) {
            int att = 0;
            do { mem_load<0, 0>(rc, lane * 16, so0, so1, am); } while (has_sent<8>(am) && ++att < SPIN);
            if (att >= SPIN) return;
            st32c(2 * s + 1, rc, fst, 0);
            tr = ck() + T_GUARD;
        }
        const int rem = (int)(tr + T_SEND - ck());
        if (rem < 0) tr -= rem;
        sled(rem >> 4);
        st32c(2 * s + 2, rc, fst, 0);
        tr += T_PERIOD;
    }
    DRAIN();
}

__device__ __forceinline__ void proj_seg(const v4u (&W)[16][4], const LAS unsigned char* X, int lane, f4 (&acc)[4][2]) {
#pragma unroll
    for (int t = 0; t < 4; ++t) { acc[t][0] = (f4){0, 0, 0, 0}; acc[t][1] = (f4){0, 0, 0, 0}; }
#pragma unroll
    for (int ks = 0; ks < 16; ++ks) {
        const v4u x0 = *(const LAS v4u*)(X + ks * 1024 + lane * 16), x1 = *(const LAS v4u*)(X + 16384 + ks * 1024 + lane * 16);
#pragma unroll
        for (int t = 0; t < 4; ++t) { acc[t][0] = mfma(W[ks][t], x0, acc[t][0]); acc[t][1] = mfma(W[ks][t], x1, acc[t][1]); }
    }
}
__device__ __forceinline__ v4u pack2(const f4& lo, const f4& hi) { return (v4u){pk(lo[0], hi[0]), pk(lo[1], hi[1]), pk(lo[2], hi[2]), pk(lo[3], hi[3])}; }
struct NatRow { __device__ constexpr int operator()(int ks, int jj) const { return 32 * ks + jj; } };
struct PairRow { __device__ constexpr int operator()(int ks, int jj) const { return 64 * (ks >> 1) + 2 * (ks & 1) + 4 * (jj >> 1) + (jj & 1); } };

__device__ __forceinline__ void kq_role(const Args& a, LAS unsigned char* L, int id, int wave, int lane) {
    const int h = id >> 1, par = id & 1, g = lane >> 4, n = lane & 15;
    const rsrc_t rc = mkrsrc(a.ws + O_COMM, 16 * BLK), rx = mkrsrc(a.ws + O_XN, 2u << 20), rk = mkrsrc(a.ws + O_KX, 16u << 20), rkm = mkrsrc(a.ws + O_KM, 4u << 20),
                 rpx = mkrsrc(a.ws + O_PX, 4u << 20), rpm = mkrsrc(a.ws + O_PM, 1u << 20);
    constexpr int XO = 0, RO = 65536;
    v4u xs[8];
    auto x_load = [&](int j) {
#pragma unroll
        for (int q = 0; q < 8; ++q) xs[q] = ld128(rx, lane * 16 + (q & 3) * 1024, (2 * j + par) * 32768 + wave * 8192 + (q >> 2) * 4096);
    };
    auto x_put = [&](int j) {
#pragma unroll
        for (int q = 0; q < 8; ++q) *(LAS v4u*)(L + XO + (j & 1) * 32768 + wave * 8192 + q * 1024 + lane * 16) = xs[q];
    };
    x_load(0);
    v4u W[16][4];
    load_w4<16>(mkrsrc(wave < 2 ? a.wk : a.wq, DM * 4096 * 2), (unsigned)(8 * g * 4096 + h * HD + 64 * (wave & 1) + 4 * n) * 2, 8192, NatRow(), W);
    x_put(0);
    x_load(1);
    BAR();
    for (int it = 0; it < 34; ++it) {
        if (it < 32) {
            f4 acc[4][2];
            proj_seg(W, L + XO + (it & 1) * 32768, lane, acc);
            LAS unsigned char* R = L + RO + (it % 3) * 16384 + (wave >> 1) * 8192 + (wave & 1) * 2048 + lane * 16;
#pragma unroll
            for (int tt = 0; tt < 2; ++tt)
#pragma unroll
                for (int y = 0; y < 2; ++y) {
                    const v4u c = pack2(acc[2 * y][tt], acc[2 * y + 1][tt]);
                    *(LAS v4u*)(R + tt * 4096 + y * 1024) = c;
                    if (wave < 2) st128c(c, rk, lane * 16 + y * 1024, ((2 * it + par) * NH + h) * 8192 + tt * 4096 + (wave & 1) * 2048);
                }
            if (it + 1 < 32) x_put(it + 1);
            DRAIN();
            if (it + 2 < 32) x_load(it + 2);
            BAR();
            if (wave == 0) st32c(it + 1, rc, lane == 0 ? C_KXF + id * 128 : OOB, 0);
        }
        if (it >= 2 && wave >= 2) {
            const int j = it - 2, s = 2 * j + par, tt = wave & 1;
            const LAS unsigned char* R = L + RO + (j % 3) * 16384 + lane * 16;
            f4 s0 = {0, 0, 0, 0}, s1 = s0, sm = s0;
            v4u q[4];
#pragma unroll
            for (int ks = 0; ks < 4; ++ks) {
                q[ks] = *(const LAS v4u*)(R + 8192 + tt * 4096 + ks * 1024);
                s0 = mfma(*(const LAS v4u*)(R + ks * 1024), q[ks], s0);
                s1 = mfma(*(const LAS v4u*)(R + 4096 + ks * 1024), q[ks], s1);
            }
            if (!wait_flags(rc, C_KMF + h * 128, s + 1)) return;
#pragma unroll
            for (int ks = 0; ks < 4; ++ks) sm = mfma(ld128c(rkm, (n & 7) * 256 + g * 16 + ks * 64, (s * NH + h) * 2048), q[ks], sm);
            const float sc = 0.08838834764831845f, l2e = 1.4426950408889634f;
            const int tok = 16 * tt + n;
            float v0[4], v1[4], vm[4], mx = -3.0e38f;
#pragma unroll
            for (int c = 0; c < 4; ++c) {
                v0[c] = (4 * g + c <= tok) ? s0[c] * sc : -1e9f;
                v1[c] = (16 + 4 * g + c <= tok) ? s1[c] * sc : -1e9f;
                vm[c] = sm[c] * sc;
                mx = fmaxf(mx, fmaxf(v0[c], fmaxf(v1[c], vm[c])));
            }
            mx = fmaxf(mx, __shfl_xor(mx, 16)); mx = fmaxf(mx, __shfl_xor(mx, 32));
            float sum = 0.f;
#pragma unroll
            for (int c = 0; c < 4; ++c) {
                v0[c] = __builtin_amdgcn_exp2f((v0[c] - mx) * l2e); v1[c] = __builtin_amdgcn_exp2f((v1[c] - mx) * l2e); vm[c] = __builtin_amdgcn_exp2f((vm[c] - mx) * l2e);
                sum += v0[c] + v1[c] + 0.5f * vm[c];
            }
            sum += __shfl_xor(sum, 16); sum += __shfl_xor(sum, 32);
            const float rs = 1.f / sum;
            st128c((v4u){pk(v0[0] * rs, v1[0] * rs), pk(v0[1] * rs, v1[1] * rs), pk(v0[2] * rs, v1[2] * rs), pk(v0[3] * rs, v1[3] * rs)}, rpx, lane * 16, ((s * NH + h) * 2 + tt) * 1024);
            st64c((v2u){pk(vm[0] * rs, vm[1] * rs), pk(vm[2] * rs, vm[3] * rs)}, rpm, g < 2 ? n * 16 + g * 8 : OOB, ((s * 2 + tt) * NH + h) * 256);
            DRAIN();
        }
        if (it >= 2) {
            BAR();
            if (wave == 2) st32c(it - 1, rc, lane == 0 ? C_PTF + id * 128 : OOB, 0);
        }
    }
    DRAIN();
}

__device__ __forceinline__ void h_role(const Args& a, LAS unsigned char* L, int id, int wave, int lane) {
    const int cg = id >> 2, cls = id & 3, g = lane >> 4, n = lane & 15;
    const rsrc_t rc = mkrsrc(a.ws + O_COMM, 16 * BLK), rx = mkrsrc(a.ws + O_XN, 2u << 20), rh = mkrsrc(a.ws + O_HH, 7u << 20);
    v4u xs[8];
    auto x_load = [&](int j) {
#pragma unroll
        for (int q = 0; q < 8; ++q) xs[q] = ld128(rx, lane * 16 + (q & 3) * 1024, (4 * j + cls) * 32768 + wave * 8192 + (q >> 2) * 4096);
    };
    auto x_put = [&](int j) {
#pragma unroll
        for (int q = 0; q < 8; ++q) *(LAS v4u*)(L + (j & 1) * 32768 + wave * 8192 + q * 1024 + lane * 16) = xs[q];
    };
    x_load(0);
    v4u W[16][4];
    load_w4<16>(mkrsrc(a.w1, DM * FF * 2), (unsigned)(8 * g * FF + 256 * cg + 64 * wave + 4 * n) * 2, FF * 2, NatRow(), W);
    x_put(0);
    x_load(1);
    BAR();
    for (int it = 0; it < 16; ++it) {
        f4 acc[4][2];
        proj_seg(W, L + (it & 1) * 32768, lane, acc);
#pragma unroll
        for (int t = 0; t < 4; ++t)
#pragma unroll
            for (int tt = 0; tt < 2; ++tt)
#pragma unroll
                for (int c = 0; c < 4; ++c) { const float v = acc[t][tt][c]; acc[t][tt][c] = v / (1.f + __expf(-v)); }
#pragma unroll
        for (int tt = 0; tt < 2; ++tt)
#pragma unroll
            for (int y = 0; y < 2; ++y) st128c(pack2(acc[2 * y][tt], acc[2 * y + 1][tt]), rh, lane * 16 + y * 1024, (((4 * it + cls) * 2 + tt) * 56 + 8 * cg + 2 * wave) * 1024);
        if (it + 1 < 16) x_put(it + 1);
        DRAIN();
        if (it + 2 < 16) x_load(it + 2);
        BAR();
        if (wave == 0) st32c(it + 1, rc, lane == 0 ? C_HF + id * 128 : OOB, 0);
    }
    DRAIN();
}

__device__ __forceinline__ void vo_role(const Args& a, LAS unsigned char* L, int id, int wave, int lane) {
    const int h = id >> 1, par = id & 1, g = lane >> 4, n = lane & 15;
    const rsrc_t rc = mkrsrc(a.ws + O_COMM, 16 * BLK);
    constexpr int VOFF = 65536;
    if (wave < 2) {
        const rsrc_t rx = mkrsrc(a.ws + O_XN, 2u << 20);
        v4u xs[16];
        auto x_load = [&](int j) {
#pragma unroll
            for (int q = 0; q < 16; ++q) xs[q] = ld128(rx, lane * 16 + (q & 3) * 1024, (2 * j + par) * 32768 + wave * 16384 + (q >> 2) * 4096);
        };
        auto x_put = [&](int j) {
#pragma unroll
            for (int q = 0; q < 16; ++q) *(LAS v4u*)(L + (j & 1) * 32768 + wave * 16384 + q * 1024 + lane * 16) = xs[q];
        };
        x_load(0);
        v4u W[16][4];
        load_w4<16>(mkrsrc(a.wv, DM * 4096 * 2), (unsigned)(8 * g * 4096 + h * HD + 64 * wave + 4 * n) * 2, 8192, NatRow(), W);
        x_put(0);
        x_load(1);
        BAR();
        for (int it = 0; it < 33; ++it) {
            if (it < 32) {
                f4 acc[4][2];
                proj_seg(W, L + (it & 1) * 32768, lane, acc);
#pragma unroll
                for (int tt = 0; tt < 2; ++tt)
#pragma unroll
                    for (int y = 0; y < 2; ++y) *(LAS v4u*)(L + VOFF + (it & 1) * 8192 + tt * 4096 + (2 * wave + y) * 1024 + lane * 16) = pack2(acc[2 * y][tt], acc[2 * y + 1][tt]);
                if (it + 1 < 32) x_put(it + 1);
                if (it + 2 < 32) x_load(it + 2);
            }
            BAR();
        }
    } else {
        const rsrc_t ro = mkrsrc(a.wo, 4096 * DM * 2), rv = mkrsrc(a.ws + O_VO, 64u << 20);
        v4u W0[4][8], W1[4][8];
        const unsigned wv = (unsigned)((h * HD + 16 * g) * DM + 256 * (wave & 1) + 8 * n) * 2;
        load_w8<4>(ro, wv, DM * 2, PairRow(), W0);
        load_w8<4>(ro, wv + 256, DM * 2, PairRow(), W1);
        const unsigned sv = (unsigned)(32 * (wave & 1) + 8 * (n >> 3)) * 16384u + h * 512 + g * 128 + (n & 7) * 16;
        BAR();
        for (int it = 0; it < 33; ++it) {
            if (it >= 1) {
                const int j = it - 1;
                v4u v0[4], v1[4];
#pragma unroll
                for (int ks = 0; ks < 4; ++ks) { v0[ks] = *(const LAS v4u*)(L + VOFF + (j & 1) * 8192 + ks * 1024 + lane * 16); v1[ks] = *(const LAS v4u*)(L + VOFF + (j & 1) * 8192 + 4096 + ks * 1024 + lane * 16); }
                const unsigned so = (unsigned)(2 * j + par) * 1048576u;
#pragma unroll
                for (int t = 0; t < 16; ++t) {
                    f4 d0 = {0, 0, 0, 0}, d1 = d0;
#pragma unroll
                    for (int ks = 0; ks < 4; ++ks) { const v4u w = t < 8 ? W0[ks][t & 7] : W1[ks][t & 7]; d0 = mfma(v0[ks], w, d0); d1 = mfma(v1[ks], w, d1); }
                    st128c(pack2(d0, d1), rv, sv, so + ((t >> 3) * 16 + (t & 7)) * 16384u);
                }
                if (j == 0 || j == 31) DRAIN(); else asm volatile("s_waitcnt vmcnt(16)" ::: "memory");
            }
            BAR();
            if (wave == 2 && it >= 1) st32c((it == 1 || it == 32) ? it : it - 1, rc, lane == 0 ? C_VOF + id * 128 : OOB, 0);
        }
    }
}

__device__ __forceinline__ void out_role(const Args& a, LAS unsigned char* L, int id, int wave, int lane) {
    const int cgp = id & 7, r8 = id >> 3, g = lane >> 4, n = lane & 15;
    const rsrc_t rc = mkrsrc(a.ws + O_COMM, 16 * BLK), rh = mkrsrc(a.ws + O_HH, 7u << 20), rv = mkrsrc(a.ws + O_VO, 64u << 20), rum = mkrsrc(a.ws + O_UM, 16u << 20),
                 rpx = mkrsrc(a.ws + O_PX, 4u << 20), rpm = mkrsrc(a.ws + O_PM, 1u << 20);
    v4u W[14][4];
    load_w4<14>(mkrsrc(a.w2, FF * DM * 2), (unsigned)((448 * wave + 16 * g) * DM + 64 * cgp + 8 * (n & 7) + 4 * (n >> 3)) * 2, DM * 2, PairRow(), W);
    const unsigned uxv = (unsigned)(8 * cgp + 4 * (n >> 3)) * 16384u + wave * 4096 + g * 128 + (n & 7) * 16;
    const unsigned umv = (unsigned)(8 * cgp + 4 * (n >> 3)) * 4096u + wave * 1024 + g * 128 + (n & 7) * 16;
    for (int u = 0; u < 8; ++u) {
        const int s = 8 * u + r8;
        f4 acc[4][2];
#pragma unroll
        for (int t = 0; t < 4; ++t) { acc[t][0] = (f4){0, 0, 0, 0}; acc[t][1] = (f4){0, 0, 0, 0}; }
        if (!wait_flags(rc, lane < 32 ? C_VOF + (lane * 2 + (s & 1)) * 128 : C_HF + ((lane < 39 ? lane - 32 : 0) * 4 + (s & 3)) * 128, lane < 32 ? (s >> 1) + 1 : (s >> 2) + 1)) return;
        {
            v4u hf[14][2];
#pragma unroll
            for (int ks = 0; ks < 14; ++ks)
#pragma unroll
                for (int tt = 0; tt < 2; ++tt) hf[ks][tt] = ld128c(rh, lane * 16 + (ks & 3) * 1024, ((s * 2 + tt) * 56 + 14 * wave + (ks & ~3)) * 1024);
#pragma unroll
            for (int ks = 0; ks < 14; ++ks)
#pragma unroll
                for (int t = 0; t < 4; ++t) { acc[t][0] = mfma(W[ks][t], hf[ks][0], acc[t][0]); acc[t][1] = mfma(W[ks][t], hf[ks][1], acc[t][1]); }
        }
        v4u ux[8][4];
#pragma unroll
        for (int hh = 0; hh < 8; ++hh)
#pragma unroll
            for (int t = 0; t < 4; ++t) ux[hh][t] = ld128c(rv, uxv + hh * 512, (unsigned)s * 1048576u + t * 16384u);
        if (!wait_flags(rc, lane < 32 ? C_PTF + (lane * 2 + (s & 1)) * 128 : C_UMF + (8 * cgp + (lane & 7)) * 128, lane < 32 ? (s >> 1) + 1 : s + 1)) return;
        {
            v4u px[8][2];
#pragma unroll
            for (int hh = 0; hh < 8; ++hh)
#pragma unroll
                for (int tt = 0; tt < 2; ++tt) px[hh][tt] = ld128c(rpx, lane * 16 + tt * 1024 + (hh & 1) * 2048, (s * NH + 8 * wave + (hh & ~1)) * 2048);
#pragma unroll
            for (int hh = 0; hh < 8; ++hh)
#pragma unroll
                for (int t = 0; t < 4; ++t) { acc[t][0] = mfma(ux[hh][t], px[hh][0], acc[t][0]); acc[t][1] = mfma(ux[hh][t], px[hh][1], acc[t][1]); }
        }
        {
            v4u pm[2][2], um[2][4];
#pragma unroll
            for (int F = 0; F < 2; ++F) {
#pragma unroll
                for (int tt = 0; tt < 2; ++tt) pm[F][tt] = ld128c(rpm, lane * 16 + F * 1024, ((s * 2 + tt) * 8 + 2 * wave) * 1024);
#pragma unroll
                for (int t = 0; t < 4; ++t) um[F][t] = ld128c(rum, umv + F * 512, (unsigned)s * 262144u + t * 4096u);
            }
#pragma unroll
            for (int F = 0; F < 2; ++F)
#pragma unroll
                for (int t = 0; t < 4; ++t) { acc[t][0] = mfma(um[F][t], pm[F][0], acc[t][0]); acc[t][1] = mfma(um[F][t], pm[F][1], acc[t][1]); }
        }
        constexpr int RS = 272;
        BAR();
#pragma unroll
        for (int tt = 0; tt < 2; ++tt)
#pragma unroll
            for (int c = 0; c < 4; ++c) {
                const int r = 4 * g + c;
                *(LAS f4*)(L + wave * (32 * RS) + (16 * tt + n) * RS + (8 * (r & 7) + 4 * (r >> 3)) * 4) = (f4){acc[0][tt][c], acc[1][tt][c], acc[2][tt][c], acc[3][tt][c]};
            }
        BAR();
        {
            const int tok = wave * 8 + (lane >> 3), c8 = lane & 7;
            f4 lo = {0, 0, 0, 0}, hi = lo;
#pragma unroll
            for (int w = 0; w < 4; ++w) { lo += *(const LAS f4*)(L + w * (32 * RS) + tok * RS + c8 * 32); hi += *(const LAS f4*)(L + w * (32 * RS) + tok * RS + c8 * 32 + 16); }
            const size_t o = (size_t)(s * 32 + tok) * DM + 64 * cgp + 8 * c8;
            const v4u xv = *(const GAS v4u*)(a.x + o);
            *(GAS v4u*)(a.out + o) = (v4u){pk(lo[0] + bflo(xv.x), lo[1] + bfhi(xv.x)), pk(lo[2] + bflo(xv.y), lo[3] + bfhi(xv.y)),
                                           pk(hi[0] + bflo(xv.z), hi[1] + bfhi(xv.z)), pk(hi[2] + bflo(xv.w), hi[3] + bfhi(xv.w))};
        }
    }
}

__global__ void __launch_bounds__(256, 1) main_kernel(Args a) {
    extern __shared__ __attribute__((aligned(16))) unsigned char lds[];
    LAS unsigned char* L = (LAS unsigned char*)lds;
    const int wave = __builtin_amdgcn_readfirstlane(threadIdx.x >> 6), lane = threadIdx.x & 63;
    if (threadIdx.x == 0) {
        GAS unsigned* claim = (GAS unsigned*)(a.ws + O_COMM + C_CLAIM);
        unsigned r = (xcc_id() & 7) * 32 + (blockIdx.x >> 3);
        for (int k = 0; k < 256; ++k, r = (r + 1) & 255)
            if (__hip_atomic_exchange(claim + r, 1u, __ATOMIC_RELAXED, __HIP_MEMORY_SCOPE_AGENT) == 0) break;
        *(LAS unsigned*)L = r;
    }
    __syncthreads();
    const int role = __builtin_amdgcn_readfirstlane(*(LAS unsigned*)L);
    __syncthreads();
    const int cls = role >> 5, bi = role & 31;
    if (cls < 4) {
        const int rep = cls * NREP + (bi & (NREP - 1));
        if (bi < 8) {
            const int head = cls * 8 + bi;
            if (wave == 0) head_wave<0>(a, L, head, lane, rep); else if (wave == 1) head_wave<1>(a, L, head, lane, rep);
            else if (wave == 2) head_wave<2>(a, L, head, lane, rep); else head_wave<3>(a, L, head, lane, rep);
        } else if (bi < 24) {
            const int sl = cls * 16 + bi - 8;
            if (wave == 0) slice_wave<0>(a, L, sl, lane, rep); else if (wave == 1) slice_wave<1>(a, L, sl, lane, rep);
            else if (wave == 2) slice_wave<2>(a, L, sl, lane, rep); else slice_wave<3>(a, L, sl, lane, rep);
        } else if (bi < 31) {
            h_role(a, L, (bi - 24) * 4 + cls, wave, lane);
        } else if (cls == 2 && wave == 0) {
            metronome(a, lane);
        }
    } else {
        const int id = (cls - 4) * 16 + (bi & 15);
        if (bi < 16) kq_role(a, L, id, wave, lane);
        else { vo_role(a, L, id, wave, lane); out_role(a, L, id, wave, lane); }
    }
}
}

extern "C" void kernel_launch(void* const* d_in, const int* in_sizes, int n_in, void* d_out, int out_size, void* d_ws, size_t ws_size, hipStream_t stream) {
    Args a;
    a.x = (const u16*)d_in[0]; a.mem0 = (const u16*)d_in[1]; a.wq = (const u16*)d_in[2]; a.wk = (const u16*)d_in[3]; a.wv = (const u16*)d_in[4]; a.wo = (const u16*)d_in[5];
    a.w1 = (const u16*)d_in[8]; a.w2 = (const u16*)d_in[9];
    a.out = (u16*)d_out;
    a.ws = (unsigned char*)(((uintptr_t)d_ws + (512u << 10) - 1) & ~(uintptr_t)((512u << 10) - 1));
    constexpr int kLds = 120 * 1024;
    (void)hipFuncSetAttribute((const void*)main_kernel, hipFuncAttributeMaxDynamicSharedMemorySize, kLds);
    prep_kernel<<<512, 256, 0, stream>>>(a);
    main_kernel<<<256, 256, kLds, stream>>>(a);
}
```

```cpp
#include <hip/hip_runtime.h>
#include <hip/hip_bf16.h>
#include <stdint.h>

#define GAS __attribute__((address_space(1)))
#define LAS __attribute__((address_space(3)))
typedef unsigned v4u __attribute__((ext_vector_type(4)));
typedef unsigned v2u __attribute__((ext_vector_type(2)));
typedef unsigned v8u __attribute__((ext_vector_type(8)));
typedef unsigned v16u __attribute__((ext_vector_type(16)));
typedef unsigned v32u __attribute__((ext_vector_type(32)));
typedef __bf16 bf8 __attribute__((ext_vector_type(8)));
typedef __bf16 bf16v __attribute__((ext_vector_type(16)));
typedef __bf16 bf2 __attribute__((ext_vector_type(2)));
typedef float f4 __attribute__((ext_vector_type(4)));
typedef float f2 __attribute__((ext_vector_type(2)));
typedef unsigned short u16;
typedef __amdgpu_buffer_rsrc_t rsrc_t;

namespace {
constexpr int SEQ = 2048, DM = 512, NH = 32, HD = 128, NSEG = 64, FF = 1792;
constexpr unsigned SENT = 0xffffffffu, OOB = 0x7fffff00u;
constexpr int BLK = 32768;
constexpr size_t O_COMM = 0;
constexpr unsigned COMM_BYTES = 20 * BLK;
constexpr size_t O_XN = 1 << 20;
constexpr size_t O_BS = O_XN + (2u << 20);
constexpr size_t O_KX = O_BS + (16u << 20);
constexpr size_t O_VO = O_KX + (16u << 20);
constexpr size_t O_UM = O_VO + (64u << 20);
constexpr size_t O_KM = O_UM + (16u << 20);
constexpr size_t O_PX = O_KM + (4u << 20);
constexpr size_t O_PM = O_PX + (4u << 20);
constexpr size_t O_HH = O_PM + (1u << 20);
constexpr size_t O_END = O_HH + (7u << 20);
constexpr unsigned C_MET = 14 * BLK;
constexpr unsigned C_PROBE = 15 * BLK;
constexpr unsigned C_MSG0 = 17 * BLK;
constexpr unsigned C_KXF = 18 * BLK, C_VOF = C_KXF + 8192, C_KMF = C_VOF + 8192, C_UMF = C_KMF + 4096, C_CFG = C_UMF + 8192;
constexpr unsigned C_INIT = 17 * BLK + 16384;
constexpr unsigned long long KEY = 0x9E3779B97F4A7C15ull;
constexpr unsigned C_NB = C_CFG + 128;
constexpr unsigned C_RDY = C_CFG + 256, C_OUTP = C_CFG + 512;
constexpr unsigned C_BF = 16 * BLK, C_XF = C_BF + 16384;
constexpr unsigned C_PTF = 19 * BLK, C_HF = C_PTF + 8192, C_CLAIM = C_HF + 4096;
constexpr int NHELP = 0;
#define T_PERIOD 564
#define T_SEND 24
#define T_DA 362
#define T_DB 140
#define T_GUARD 170
#define T_DU 200
#define T_SENDB 0
#define T_DMS 15
#define UXEARLY 0
#define ROTW 0
#define T_DK 12
#define T_DQ 24
#define SLOWNAP 12
#define T_GH 24
#define T_GS 22
#define T_DECAY 8
#define BNAP 6
constexpr int SPIN = 1 << 17;

#define DG(slot, val) do {} while (0)
#define DGT(slot) do {} while (0)
#define BARW(accum) BAR()
#define STDECL
#define STAMP(k)
#define STAMPR(k)
#define STFLUSH
#define STDUMP
#define DGARG
#define DGPASS
struct Args {
    const u16* x; const u16* mem0; const u16* wq; const u16* wk; const u16* wv; const u16* wo; const u16* w1; const u16* w2;
    u16* out; unsigned char* ws;
    int period, send, da, db, guard, du, sendb, dms, dk, dq, gh, gs, decay, pad;
};

__device__ __forceinline__ rsrc_t mkrsrc(const void* p, unsigned bytes) { return __builtin_amdgcn_make_buffer_rsrc((void*)p, 0, bytes, 0x00020000); }
__device__ __forceinline__ unsigned ck() { return (unsigned)__builtin_amdgcn_s_memtime(); }
__device__ __forceinline__ unsigned xcc_id() { return (unsigned)__builtin_amdgcn_s_getreg(20 | (3 << 11)) & 0xfu; }
__device__ __forceinline__ unsigned pk(float lo, float hi) { f2 v = {lo, hi}; bf2 r = __builtin_convertvector(v, bf2); return __builtin_bit_cast(unsigned, r); }
__device__ __forceinline__ float bflo(unsigned w) { return __builtin_bit_cast(float, w << 16); }
__device__ __forceinline__ float bfhi(unsigned w) { return __builtin_bit_cast(float, w & 0xffff0000u); }
__device__ __forceinline__ float rbf(float v) { return bflo(pk(v, v)); }
template <int CTRL> __device__ __forceinline__ float dppf(float v) { return __builtin_bit_cast(float, __builtin_amdgcn_update_dpp(0, __builtin_bit_cast(int, v), CTRL, 0xf, 0xf, true)); }
__device__ __forceinline__ bf16v cat(v4u a, v4u b) { v8u r = {a.x, a.y, a.z, a.w, b.x, b.y, b.z, b.w}; return __builtin_bit_cast(bf16v, r); }
__device__ __forceinline__ f4 mfma(v4u a, v4u b, f4 c) { return __builtin_amdgcn_mfma_f32_16x16x32_bf16(__builtin_bit_cast(bf8, a), __builtin_bit_cast(bf8, b), c, 0, 0, 0); }
__device__ __forceinline__ f4 smfma(v4u a, v4u b0, v4u b1, f4 c, int idx) { return __builtin_amdgcn_smfmac_f32_16x16x64_bf16(__builtin_bit_cast(bf8, a), cat(b0, b1), c, idx, 0, 0); }
__device__ __forceinline__ v4u ld128(rsrc_t r, unsigned v, unsigned s) { return __builtin_amdgcn_raw_buffer_load_b128(r, v, s, 0); }
#define WAUX 0
__device__ __forceinline__ v4u ld128w(rsrc_t r, unsigned v, unsigned s) { return __builtin_amdgcn_raw_buffer_load_b128(r, v, s, WAUX); }
__device__ __forceinline__ v4u ld128c(rsrc_t r, unsigned v, unsigned s) { return __builtin_amdgcn_raw_buffer_load_b128(r, v, s, 16); }
__device__ __forceinline__ void st128r(v4u d, rsrc_t r, unsigned v, unsigned s) { __builtin_amdgcn_raw_buffer_store_b128(d, r, v, s, 16); }
__device__ __forceinline__ void st128c(v4u d, rsrc_t r, unsigned v, unsigned s) { __builtin_amdgcn_raw_buffer_store_b128(d, r, v, s, 16); asm volatile("s_nop 1" :: "v"(d) : "memory"); }
#define BAUX 16
__device__ __forceinline__ v4u ld128b(rsrc_t r, unsigned v, unsigned s) { return __builtin_amdgcn_raw_buffer_load_b128(r, v, s, BAUX); }
__device__ __forceinline__ void st64c(v2u d, rsrc_t r, unsigned v, unsigned s) { __builtin_amdgcn_raw_buffer_store_b64(d, r, v, s, 16); }
__device__ __forceinline__ void st32c(unsigned d, rsrc_t r, unsigned v, unsigned s) { __builtin_amdgcn_raw_buffer_store_b32(d, r, v, s, 16); }
#define MMA_WA0(d, w, x) asm volatile("v_mfma_f32_16x16x32_bf16 %0, %1, %2, 0" : "=&v"(d) : "a"(w), "v"(x))
#define MMA_WA(d, w, x) asm volatile("v_mfma_f32_16x16x32_bf16 %0, %1, %2, %0" : "+v"(d) : "a"(w), "v"(x))
#define MMA_WB0(d, x, w) asm volatile("v_mfma_f32_16x16x32_bf16 %0, %1, %2, 0" : "=&v"(d) : "v"(x), "a"(w))
#define MMA_WB(d, x, w) asm volatile("v_mfma_f32_16x16x32_bf16 %0, %1, %2, %0" : "+v"(d) : "v"(x), "a"(w))
#define MMA_VV(d, x, y) asm volatile("v_mfma_f32_16x16x32_bf16 %0, %1, %2, %0" : "+v"(d) : "v"(x), "v"(y))
#define BAR() asm volatile("s_waitcnt lgkmcnt(0)\n\ts_barrier" ::: "memory")
#define DRAIN() asm volatile("s_waitcnt vmcnt(0)" ::: "memory")
#define FENCE() asm volatile("" ::: "memory")

__device__ __forceinline__ void sled(int n) {
    const int m = __builtin_amdgcn_readfirstlane(min(max(n, 0), 256));
    asm volatile("s_getpc_b64 vcc\n\ts_add_u32 vcc_lo, vcc_lo, %0\n\ts_addc_u32 vcc_hi, vcc_hi, 0\n\ts_setpc_b64 vcc\n\t.rept 256\n\ts_nop 3\n\t.endr" :: "s"(12 + (256 - m) * 4) : "scc", "vcc", "memory");
}
template <int N> __device__ __forceinline__ void pause16() { asm volatile(".rept %0\n\ts_nop 3\n\t.endr" :: "i"(N) : "memory"); }
__device__ __forceinline__ unsigned now24() { return (unsigned)__builtin_amdgcn_s_memrealtime() << 10; }
constexpr int U = 256;
#define TSTEP "s_memrealtime vcc\n\ts_waitcnt lgkmcnt(0)\n\ts_lshl_b32 %[d], vcc_lo, 10\n\ts_sub_i32 %[d], %[t], %[d]\n\ts_cmp_lt_i32 %[d], %[thr]\n\ts_cbranch_scc1 2f\n\t"
__device__ __forceinline__ void wait_until(unsigned t) {
    int d, n = 2048;
    asm volatile("1:\n\t" TSTEP TSTEP TSTEP TSTEP TSTEP TSTEP
                 "s_sub_u32 %[n], %[n], 1\n\ts_cmp_lg_u32 %[n], 0\n\ts_cbranch_scc1 1b\n\t"
                 "2:\n\t"
                 "s_ashr_i32 %[d], %[d], 8\n\ts_sub_i32 %[d], 30, %[d]\n\ts_mul_i32 %[d], %[d], 47\n\ts_ashr_i32 %[d], %[d], 5\n\t"
                 "s_max_i32 %[d], %[d], 0\n\ts_min_i32 %[d], %[d], 24\n\ts_lshl2_add_u32 %[d], %[d], 12\n\t"
                 "s_getpc_b64 vcc\n\ts_add_u32 vcc_lo, vcc_lo, %[d]\n\ts_addc_u32 vcc_hi, vcc_hi, 0\n\ts_setpc_b64 vcc\n\t.rept 24\n\ts_nop 0\n\t.endr"
                 : [d] "=&s"(d), [n] "+s"(n) : [t] "s"(t & ~255u), [thr] "s"(30 * U) : "scc", "vcc", "memory");
}
__device__ __forceinline__ void nap(int n) { for (int i = 0; i < n; ++i) __builtin_amdgcn_s_sleep(8); }
__device__ __forceinline__ v2u rfl2(v2u v) { return (v2u){(unsigned)__builtin_amdgcn_readfirstlane(v.x), (unsigned)__builtin_amdgcn_readfirstlane(v.y)}; }
__device__ __forceinline__ v2u ld_word(rsrc_t rc, unsigned voff) { return __builtin_amdgcn_raw_buffer_load_b64(rc, voff, 0, 16); }
__device__ __forceinline__ v2u wait_word(rsrc_t rc, unsigned voff, unsigned want, v2u first) {
    v2u v = rfl2(first);
    for (int i = 0; (v.x & 255u) < want; ++i) {
        if (i > SPIN * 4) return (v2u){0u, 0u};
        FENCE();
        v = rfl2(ld_word(rc, voff));
    }
    FENCE();
    return v;
}
template <int NAP = 2>
__device__ __forceinline__ bool wait_flags(rsrc_t rc, unsigned voff, unsigned want) {
    for (int i = 0; i < SPIN * 4; ++i) {
        FENCE();
        const unsigned v = __builtin_amdgcn_raw_buffer_load_b32(rc, voff, 0, 16);
        if (__builtin_amdgcn_ballot_w64(v < want) == 0) { FENCE(); return true; }
        __builtin_amdgcn_s_sleep(NAP);
    }
    return false;
}

__device__ __forceinline__ v4u init_ld(rsrc_t rc) { return ld128c(rc, C_INIT + (blockIdx.x & 31) * 128, 0); }
__device__ __forceinline__ bool wait_init(rsrc_t rc, v4u v) {
    for (int i = 0; i < SPIN * 4; ++i) {
        const unsigned long long e = v.x | ((unsigned long long)v.y << 32), k = v.z | ((unsigned long long)v.w << 32);
        if (__builtin_amdgcn_ballot_w64(k - e != KEY) == 0) { FENCE(); return true; }
        __builtin_amdgcn_s_sleep(4);
        FENCE();
        v = init_ld(rc);
    }
    return false;
}
__device__ __forceinline__ void reset_words(rsrc_t rc, int tid) {
    const v2u e = ld_word(rc, C_INIT + (tid & 31) * 128);
    const v4u z = {0u, 0u, 0u, 0u};
#pragma unroll
    for (int k = 0; k < 18; ++k) st128r(z, rc, tid * 16, (k < 7 ? 16 * BLK : k < 15 ? 18 * BLK - 7 * 4096 : 19 * BLK - 15 * 4096) + k * 4096);
    DRAIN();
    BAR();
    const unsigned long long k = (e.x | ((unsigned long long)e.y << 32)) + KEY;
    st64c((v2u){(unsigned)k, (unsigned)(k >> 32)}, rc, tid < 32 ? C_INIT + tid * 128 + 8 : OOB, 0);
}
__device__ __forceinline__ bool wait_flags2(rsrc_t rc, unsigned voff, unsigned want) {
    for (int i = 0; i < SPIN * 4; ++i) {
        FENCE();
        const v2u v = __builtin_amdgcn_raw_buffer_load_b64(rc, voff, 0, 16);
        if (__builtin_amdgcn_ballot_w64(min(v.x, v.y) < want) == 0) { FENCE(); return true; }
        __builtin_amdgcn_s_sleep(2);
    }
    return false;
}

#define SCHED() __builtin_amdgcn_sched_barrier(0)
template <bool PIN> __device__ __forceinline__ void pin(v4u& v) { if (PIN) asm volatile("" : "+a"(v)); }
struct NoHook { __device__ void operator()(int) const {} };
template <int KS, int AHEAD, bool PIN, class RowF, class Hook = NoHook>
__device__ __forceinline__ void load_w4(rsrc_t r, unsigned voff, unsigned rowbytes, RowF rowf, v4u (&W)[KS][4], Hook hook = Hook()) {
    v2u R[KS][8];
#pragma unroll
    for (int ks = 0; ks < KS + AHEAD; ++ks) {
        if (ks < KS) {
#pragma unroll
            for (int jj = 0; jj < 8; ++jj) R[ks][jj] = __builtin_amdgcn_raw_buffer_load_b64(r, voff, rowf(ks, jj) * rowbytes, WAUX);
        }
        SCHED();
        if (ks >= AHEAD) {
            const int k = ks - AHEAD;
#pragma unroll
            for (int t = 0; t < 4; ++t) {
#pragma unroll
                for (int q = 0; q < 4; ++q) W[k][t][q] = __builtin_amdgcn_perm(R[k][2 * q + 1][t >> 1], R[k][2 * q][t >> 1], (t & 1) ? 0x07060302u : 0x05040100u);
                pin<PIN>(W[k][t]);
            }
        }
        SCHED();
        hook(ks);
        SCHED();
    }
}
template <int KS, int AHEAD, bool PIN, class RowF, class Hook = NoHook>
__device__ __forceinline__ void load_w8(rsrc_t r, unsigned voff, unsigned rowbytes, RowF rowf, v4u (&W)[KS][8], Hook hook = Hook()) {
    v4u R[KS][8];
#pragma unroll
    for (int ks = 0; ks < KS + AHEAD; ++ks) {
        if (ks < KS) {
#pragma unroll
            for (int jj = 0; jj < 8; ++jj) R[ks][jj] = ld128w(r, voff, rowf(ks, jj) * rowbytes);
        }
        SCHED();
        if (ks >= AHEAD) {
            const int k = ks - AHEAD;
#pragma unroll
            for (int t = 0; t < 8; ++t) {
#pragma unroll
                for (int q = 0; q < 4; ++q) W[k][t][q] = __builtin_amdgcn_perm(R[k][2 * q + 1][t >> 1], R[k][2 * q][t >> 1], (t & 1) ? 0x07060302u : 0x05040100u);
                pin<PIN>(W[k][t]);
            }
        }
        SCHED();
        hook(ks);
        SCHED();
    }
}
template <int AHEAD>
__device__ __forceinline__ void load_w4s(rsrc_t r, unsigned voff, int rot, v8u (&W)[8][4]) {
    v2u R[8][16];
#pragma unroll
    for (int f = 0; f < 8 + AHEAD; ++f) {
        if (f < 8) {
            const unsigned so = ((f + rot) & 7) * 64 * 8192;
#pragma unroll
            for (int j = 0; j < 16; ++j) {
                const int jj = j & 7;
                R[f][j] = __builtin_amdgcn_raw_buffer_load_b64(r, voff, so + (4 * (j >> 3) + 16 * (jj >> 2) + 8 * (jj & 1) + ((jj >> 1) & 1)) * 8192, WAUX);
            }
        }
        SCHED();
        if (f >= AHEAD) {
            const int k = f - AHEAD;
#pragma unroll
            for (int t = 0; t < 4; ++t) {
#pragma unroll
                for (int q = 0; q < 8; ++q) W[k][t][q] = __builtin_amdgcn_perm(R[k][2 * q + 1][t >> 1], R[k][2 * q][t >> 1], (t & 1) ? 0x07060302u : 0x05040100u);
                asm volatile("" : "+a"(W[k][t]));
            }
        }
        SCHED();
    }
}

template <int NG> __device__ __forceinline__ void ln_load(const Args& a, int grp0, int wave, int lane, v4u (&xv)[2 * NG]) {
#pragma unroll
    for (int k = 0; k < 2 * NG; ++k) xv[k] = *(const GAS v4u*)(a.x + (size_t)((grp0 + (k >> 1)) * 8 + wave * 2 + (k & 1)) * DM + lane * 8);
}
__device__ __forceinline__ float wave_sum(float s) {
    s += dppf<0x128>(s); s += dppf<0x124>(s); s += dppf<0x122>(s); s += dppf<0x121>(s);
    s += __shfl_xor(s, 16); s += __shfl_xor(s, 32);
    return s;
}
template <int NG> __device__ __forceinline__ void ln_store(const Args& a, LAS unsigned char* L, int grp0, int wave, int lane, const v4u (&xv)[2 * NG]) {
    const rsrc_t rx = mkrsrc(a.ws + O_XN, 2u << 20);
#pragma unroll
    for (int k = 0; k < 2 * NG; ++k) {
        const v4u v = xv[k];
        float e[8] = {bflo(v.x), bfhi(v.x), bflo(v.y), bfhi(v.y), bflo(v.z), bfhi(v.z), bflo(v.w), bfhi(v.w)};
        float s = 0.f;
#pragma unroll
        for (int q = 0; q < 8; ++q) s += e[q];
        const float mean = wave_sum(s) * (1.f / DM);
        float ss = 0.f;
#pragma unroll
        for (int q = 0; q < 8; ++q) { const float c = e[q] - mean; ss += c * c; }
        const float mb = rbf(mean), sd = rbf(__builtin_amdgcn_sqrtf(rbf(rbf(wave_sum(ss) * (1.f / DM)) + 1e-5f))), rs = __builtin_amdgcn_rcpf(sd);
#pragma unroll
        for (int q = 0; q < 8; ++q) { const float c = rbf(e[q] - mb), y = c * rs; e[q] = __builtin_fmaf(__builtin_fmaf(-y, sd, c), rs, y); }
        const v4u o = {pk(e[0], e[1]), pk(e[2], e[3]), pk(e[4], e[5]), pk(e[6], e[7])};
        *(LAS v4u*)(L + (k >> 1) * 9216 + lane * 144 + (wave * 2 + (k & 1)) * 16) = o;
    }
    BAR();
#pragma unroll
    for (int k = 0; k < 2 * NG; ++k) {
        const int grp = grp0 + (k >> 1), ch = 16 * wave + 8 * (k & 1) + (lane >> 3);
        st128c(*(const LAS v4u*)(L + (k >> 1) * 9216 + ch * 144 + (lane & 7) * 16), rx, ch * 256 + (lane & 7) * 16, (grp >> 1) * 16384 + (grp & 1) * 128);
    }
    FENCE();
}
template <int AT, int NY> struct LnHook {
    const Args& a; LAS unsigned char* L; int id, wave, lane; const v4u (&xv)[2]; v4u iv;
    __device__ __forceinline__ void operator()(int stage) {
        if (stage == AT) { ln_store<1>(a, L, id, wave, lane, xv); iv = init_ld(mkrsrc(a.ws + O_COMM, COMM_BYTES)); FENCE(); }
        if (stage == AT + NY / 8) {
            asm volatile("s_waitcnt vmcnt(%0)" :: "i"(NY) : "memory");
            wait_init(mkrsrc(a.ws + O_COMM, COMM_BYTES), iv);
            st32c(1, mkrsrc(a.ws + O_COMM, COMM_BYTES), lane == 0 ? C_XF + id * 128 + wave * 4 : OOB, 0);
        }
    }
};
__device__ __forceinline__ void ln_rest(const Args& a, LAS unsigned char* L, int id, int wave, int lane) {
    nap(a.sendb);
    for (int k = 0; k < 4; ++k) {
        v4u xv[6];
        ln_load<3>(a, 64 + 12 * id + 3 * k, wave, lane, xv);
        ln_store<3>(a, L + (k & 1) * 32768, 64 + 12 * id + 3 * k, wave, lane, xv);
    }
    DRAIN();
    st32c(1, mkrsrc(a.ws + O_COMM, COMM_BYTES), lane == 0 ? C_XF + 8192 + id * 128 + wave * 4 : OOB, 0);
}
__device__ __forceinline__ bool wait_xn(rsrc_t rc, int, int lane, bool all) {
    for (int i = 0; i < SPIN; ++i) {
        FENCE();
        const v4u p = ld128c(rc, all ? C_XF + 8192 + (lane & 15) * 128 : C_XF + lane * 128, 0);
        if (__builtin_amdgcn_ballot_w64((p.x & p.y & p.z & p.w) == 0) == 0) { FENCE(); return true; }
        __builtin_amdgcn_s_sleep(SLOWNAP);
    }
    return false;
}

template <int NT, class Hook>
__device__ __forceinline__ void make_b(const Args& a, LAS unsigned char* L, int h, int f0, int maker, int wave, int lane, Hook hook DGARG) {
    const int g = lane >> 4, n = lane & 15;
    const rsrc_t rv = mkrsrc(a.wv, DM * 4096 * 2), ro = mkrsrc(a.wo, 4096 * DM * 2), rb = mkrsrc(a.ws + O_BS, 16u << 20);
    v4u S[4 * NT];
#pragma unroll
    for (int q = 0; q < 4 * NT; ++q) S[q] = ld128w(rv, (unsigned)((64 * f0 + 4 * wave + g) * 4096 + h * HD + 8 * n) * 2, q * 16 * 8192);
    SCHED();
    const int rot = ROTW ? f0 >> 1 : 0;
    v4u W[4][8];
    load_w8<4, 4, true>(ro, (unsigned)((h * HD + 8 * g) * DM + 128 * wave + 8 * n) * 2, DM * 2, [&](int ks, int jj) { return 32 * ((ks + rot) & 3) + jj; }, W, [&](int stage) {
        hook(stage);
        if (stage == 3) {
#pragma unroll
            for (int q = 0; q < 4 * NT; ++q) *(LAS v4u*)(L + (16 * q + 4 * wave + g) * 256 + 16 * (n ^ g ^ ((wave >> 1) << 2) ^ (((q >> 1) & 1) << 3))) = S[q];
        }
    });
    DGT(20);
    BAR();
    const int gg = n >> 2, c = n & 3;
    const int sw = (c >> 1) | ((gg >> 1) << 1) | ((c & 1) << 2) | ((gg & 1) << 3);
    const LAS unsigned char* al[4];
#pragma unroll
    for (int ks = 0; ks < 4; ++ks) al[ks] = L + (8 * (4 * (gg & 1) + (c & 1)) + 2 * (gg >> 1) + (c >> 1)) * 256 + 16 * ((4 * ((ks + rot) & 3) + g) ^ sw);
    v4u A[NT][4][4];
    auto a_read = [&](int q, int ks) {
#pragma unroll
        for (int dt = 0; dt < 4; ++dt) A[q][dt][ks] = *(const LAS v4u*)(al[ks] + (64 * q + 16 * (dt & 1) + 4 * (dt >> 1)) * 256);
    };
#pragma unroll
    for (int ks = 0; ks < 4; ++ks) a_read(0, ks);
    const unsigned vo = (unsigned)(16 * wave + 8 * (n >> 3)) * 262144u + (h >> 3) * 65536u + f0 * 8192u + (h & 7) * 1024u + g * 128u + (n & 7) * 16u;
    const rsrc_t rc = mkrsrc(a.ws + O_COMM, COMM_BYTES);
    const unsigned fv = lane == 0 ? C_BF + maker * 128 + wave * 4 : OOB;
    const v4u iv = init_ld(rc);
    f4 d[8 * NT][4];
    v4u o[2];
#pragma unroll
    for (int T = 0; T <= 8 * NT; ++T) {
        const int q = T >> 3, t = T & 7, u = T - 1;
        if (T == 8 * NT) asm volatile("s_nop 7" : "+v"(d[u][0]), "+v"(d[u][1]), "+v"(d[u][2]), "+v"(d[u][3]));
#pragma unroll
        for (int m = 0; m < 16; ++m) {
            const int ks = m >> 2, dt = m & 3;
            SCHED();
            if (T < 8 * NT) { if (ks == 0) MMA_WB0(d[T][dt], A[q][dt][0], W[0][t]); else MMA_WB(d[T][dt], A[q][dt][ks], W[ks][t]); }
            SCHED();
            if (m == 1 && t < 4 && q + 1 < NT) a_read(q + 1, t);
            if (u >= 0 && m >= 6) {
                const int k = m - 6, hi = k / 5, e = k % 5;
                if (k == 0) asm volatile("" : "+v"(d[u][0]), "+v"(d[u][1]));
                if (k == 5) asm volatile("" : "+v"(d[u][2]), "+v"(d[u][3]));
                if (e < 4) o[hi][e] = pk(d[u][2 * hi + (e >> 1)][2 * (e & 1)], d[u][2 * hi + (e >> 1)][2 * (e & 1) + 1]);
                else st128c(o[hi], rb, vo + hi * 512, (u & 7) * 262144u + (u >> 3) * 8192u);
            }
        }
        if (t == 7 && q < 3) DGT(21 + q);
    }
    DRAIN();
    DGT(24);
    wait_init(rc, iv);
    st32c(NT, rc, fv, 0);
    BAR();
}

#define CORE_LOAD "buffer_load_dwordx4 v[128:131], %[o0], %[r], %[so] offen sc1\n\t" \
        "v_mov_b64 v[160:161], 0\n\tv_mov_b64 v[162:163], 0\n\ts_nop 3\n\t" \
        "buffer_load_dwordx4 v[132:135], %[o0], %[r], %[so] offen offset:1024 sc1\n\t" \
        "v_mov_b64 v[164:165], 0\n\tv_mov_b64 v[166:167], 0\n\ts_nop 3\n\t" \
        "buffer_load_dwordx4 v[136:139], %[o1], %[r], %[so] offen sc1\n\t" \
        "v_mov_b64 v[168:169], 0\n\tv_mov_b64 v[170:171], 0\n\ts_nop 3\n\t" \
        "buffer_load_dwordx4 v[140:143], %[o1], %[r], %[so] offen offset:1024 sc1\n\t" \
        "v_mov_b64 v[172:173], 0\n\tv_mov_b64 v[174:175], 0\n\ts_nop 3\n\t" \
        "buffer_load_dwordx4 v[144:147], %[o2], %[r], %[so] offen sc1\n\t" \
        "s_nop 7\n\t" \
        "buffer_load_dwordx4 v[148:151], %[o2], %[r], %[so] offen offset:1024 sc1\n\t" \
        "s_nop 7\n\t" \
        "buffer_load_dwordx4 v[152:155], %[o3], %[r], %[so] offen sc1\n\t" \
        "s_nop 7\n\t" \
        "buffer_load_dwordx4 v[156:159], %[o3], %[r], %[so] offen offset:1024 sc1\n\t"
#define CORE_MMA(N0, MID, N) "s_waitcnt vmcnt(" N0 "+7)\n\t" \
        "v_smfmac_f32_16x16x64_bf16 v[160:163], v[128:131], %[w0], %[idx]\n\t" \
        "v_smfmac_f32_16x16x64_bf16 v[164:167], v[128:131], %[w1], %[idx]\n\t" \
        "v_smfmac_f32_16x16x64_bf16 v[168:171], v[128:131], %[w2], %[idx]\n\t" \
        "v_smfmac_f32_16x16x64_bf16 v[172:175], v[128:131], %[w3], %[idx]\n\t" \
        "s_waitcnt vmcnt(" N0 "+6)\n\t" \
        "v_smfmac_f32_16x16x64_bf16 v[160:163], v[132:135], %[w4], %[idx]\n\t" \
        "v_max_u32 v176, v128, v132\n\tv_cmp_eq_u32 vcc, -1, v176\n\t" \
        "v_smfmac_f32_16x16x64_bf16 v[164:167], v[132:135], %[w5], %[idx]\n\t" \
        "v_smfmac_f32_16x16x64_bf16 v[168:171], v[132:135], %[w6], %[idx]\n\t" \
        "v_smfmac_f32_16x16x64_bf16 v[172:175], v[132:135], %[w7], %[idx]\n\t" \
        MID \
        "s_cbranch_vccnz 8f\n\t" \
        "s_waitcnt vmcnt(" N "+5)\n\t" \
        "v_smfmac_f32_16x16x64_bf16 v[160:163], v[136:139], %[w8], %[idx]\n\t" \
        "v_smfmac_f32_16x16x64_bf16 v[164:167], v[136:139], %[w9], %[idx]\n\t" \
        "v_smfmac_f32_16x16x64_bf16 v[168:171], v[136:139], %[w10], %[idx]\n\t" \
        "v_smfmac_f32_16x16x64_bf16 v[172:175], v[136:139], %[w11], %[idx]\n\t" \
        "s_waitcnt vmcnt(" N "+4)\n\t" \
        "v_smfmac_f32_16x16x64_bf16 v[160:163], v[140:143], %[w12], %[idx]\n\t" \
        "v_max_u32 v176, v136, v140\n\tv_cmp_eq_u32 vcc, -1, v176\n\t" \
        "v_smfmac_f32_16x16x64_bf16 v[164:167], v[140:143], %[w13], %[idx]\n\t" \
        "v_smfmac_f32_16x16x64_bf16 v[168:171], v[140:143], %[w14], %[idx]\n\t" \
        "v_smfmac_f32_16x16x64_bf16 v[172:175], v[140:143], %[w15], %[idx]\n\t" \
        "s_cbranch_vccnz 8f\n\t" \
        "s_waitcnt vmcnt(" N "+3)\n\t" \
        "v_smfmac_f32_16x16x64_bf16 v[160:163], v[144:147], %[w16], %[idx]\n\t" \
        "v_smfmac_f32_16x16x64_bf16 v[164:167], v[144:147], %[w17], %[idx]\n\t" \
        "v_smfmac_f32_16x16x64_bf16 v[168:171], v[144:147], %[w18], %[idx]\n\t" \
        "v_smfmac_f32_16x16x64_bf16 v[172:175], v[144:147], %[w19], %[idx]\n\t" \
        "s_waitcnt vmcnt(" N "+2)\n\t" \
        "v_smfmac_f32_16x16x64_bf16 v[160:163], v[148:151], %[w20], %[idx]\n\t" \
        "v_max_u32 v176, v144, v148\n\tv_cmp_eq_u32 vcc, -1, v176\n\t" \
        "v_smfmac_f32_16x16x64_bf16 v[164:167], v[148:151], %[w21], %[idx]\n\t" \
        "v_smfmac_f32_16x16x64_bf16 v[168:171], v[148:151], %[w22], %[idx]\n\t" \
        "v_smfmac_f32_16x16x64_bf16 v[172:175], v[148:151], %[w23], %[idx]\n\t" \
        "s_cbranch_vccnz 8f\n\t" \
        "s_waitcnt vmcnt(" N "+1)\n\t" \
        "v_smfmac_f32_16x16x64_bf16 v[160:163], v[152:155], %[w24], %[idx]\n\t" \
        "v_smfmac_f32_16x16x64_bf16 v[164:167], v[152:155], %[w25], %[idx]\n\t" \
        "s_waitcnt vmcnt(" N ")\n\t" \
        "v_smfmac_f32_16x16x64_bf16 v[160:163], v[156:159], %[w28], %[idx]\n\t" \
        "v_smfmac_f32_16x16x64_bf16 v[164:167], v[156:159], %[w29], %[idx]\n\t" \
        "v_smfmac_f32_16x16x64_bf16 v[168:171], v[152:155], %[w26], %[idx]\n\t" \
        "v_smfmac_f32_16x16x64_bf16 v[172:175], v[152:155], %[w27], %[idx]\n\t" \
        "v_smfmac_f32_16x16x64_bf16 v[168:171], v[156:159], %[w30], %[idx]\n\t" \
        "v_smfmac_f32_16x16x64_bf16 v[172:175], v[156:159], %[w31], %[idx]\n\t"
#define CORE_FOLD_HEAD   \
        "s_nop 2\n\tv_pk_add_f32 v[176:177], v[160:161], v[162:163]\n\tv_pk_add_f32 v[178:179], v[164:165], v[166:167]\n\t" \
        "v_cmp_u_f32 vcc, v176, v177\n\tv_cvt_pk_bf16_f32 v180, v176, v178\n\tv_cvt_pk_bf16_f32 v181, v177, v179\n\t" \
        "v_pk_add_f32 v[176:177], v[168:169], v[170:171]\n\tv_pk_add_f32 v[178:179], v[172:173], v[174:175]\n\t" \
        "ds_write2_b32 %[dst], v180, v181 offset1:72\n\t" \
        "v_cvt_pk_bf16_f32 v182, v176, v178\n\tv_cvt_pk_bf16_f32 v183, v177, v179\n\t" \
        "ds_write2_b32 %[dst], v182, v183 offset0:16 offset1:88\n\t"
#define CORE_FOLD_SLICE   \
        "s_nop 2\n\tv_pk_add_f32 v[176:177], v[160:161], v[162:163]\n\tv_pk_add_f32 v[178:179], v[164:165], v[166:167]\n\t" \
        "v_cmp_u_f32 vcc, v176, v177\n\tv_cvt_pk_bf16_f32 v180, v176, v177\n\tv_cvt_pk_bf16_f32 v181, v178, v179\n\t" \
        "v_pk_add_f32 v[176:177], v[168:169], v[170:171]\n\tv_pk_add_f32 v[178:179], v[172:173], v[174:175]\n\t" \
        "ds_write2st64_b32 %[dst], v180, v181 offset1:1\n\t" \
        "v_cvt_pk_bf16_f32 v182, v176, v177\n\tv_cvt_pk_bf16_f32 v183, v178, v179\n\t" \
        "ds_write2st64_b32 %[dst], v182, v183 offset0:2 offset1:3\n\t"
#define CORE_TAIL(N, AGAIN) "s_cbranch_vccz 9f\n\t" "v_max3_u32 v176, v128, v129, v130\n\tv_max3_u32 v176, v176, v131, v132\n\tv_max3_u32 v176, v176, v133, v134\n\tv_max3_u32 v176, v176, v135, v136\n\tv_max3_u32 v176, v176, v137, v138\n\tv_max3_u32 v176, v176, v139, v140\n\tv_max3_u32 v176, v176, v141, v142\n\tv_max3_u32 v176, v176, v143, v144\n\tv_max3_u32 v176, v176, v145, v146\n\tv_max3_u32 v176, v176, v147, v148\n\tv_max3_u32 v176, v176, v149, v150\n\tv_max3_u32 v176, v176, v151, v152\n\tv_max3_u32 v176, v176, v153, v154\n\tv_max3_u32 v176, v176, v155, v156\n\tv_max3_u32 v176, v176, v157, v158\n\tv_max_u32 v176, v176, v159\n\t" \
        "v_cmp_eq_u32 vcc, -1, v176\n\ts_cbranch_vccz 9f\n\t8:\n\ts_add_u32 %[bad], %[bad], 1\n\ts_nop 6\n\t" AGAIN "s_waitcnt vmcnt(" N ")\n\t9:"
#define CORE_AGAIN "s_cmp_lt_u32 %[bad], 0x20000\n\ts_cbranch_scc1 7b\n\t"
#define CORE_OUT [bad] "+s"(bad), "=&{v[128:159]}"(t0), "=&{v[160:175]}"(t1), "=&{v[176:183]}"(t2)
#define CORE_IN [w0] "a"(W[0][0]), [w1] "a"(W[0][1]), [w2] "a"(W[0][2]), [w3] "a"(W[0][3]), [w4] "a"(W[1][0]), [w5] "a"(W[1][1]), [w6] "a"(W[1][2]), [w7] "a"(W[1][3]), [w8] "a"(W[2][0]), [w9] "a"(W[2][1]), [w10] "a"(W[2][2]), [w11] "a"(W[2][3]), [w12] "a"(W[3][0]), [w13] "a"(W[3][1]), [w14] "a"(W[3][2]), [w15] "a"(W[3][3]), [w16] "a"(W[4][0]), [w17] "a"(W[4][1]), [w18] "a"(W[4][2]), [w19] "a"(W[4][3]), [w20] "a"(W[5][0]), [w21] "a"(W[5][1]), [w22] "a"(W[5][2]), [w23] "a"(W[5][3]), [w24] "a"(W[6][0]), [w25] "a"(W[6][1]), [w26] "a"(W[6][2]), [w27] "a"(W[6][3]), [w28] "a"(W[7][0]), [w29] "a"(W[7][1]), [w30] "a"(W[7][2]), [w31] "a"(W[7][3]), \
        [idx] "v"(idx), [dst] "v"(dst), [r] "s"(r), [so] "s"(so), [o0] "v"(o[0]), [o1] "v"(o[1]), [o2] "v"(o[2]), [o3] "v"(o[3])
template <bool HEAD>
__device__ __forceinline__ int core(rsrc_t r, unsigned so, const unsigned (&o)[4], const v8u (&W)[8][4], int idx, const LAS unsigned char* dst) {
    int bad = 0; v32u t0; v16u t1; v8u t2;
    if (HEAD) asm volatile("7:\n\t" CORE_LOAD CORE_MMA("0", "", "0") CORE_FOLD_HEAD CORE_TAIL("0", CORE_AGAIN) : CORE_OUT : CORE_IN : "vcc", "scc", "memory");
    else asm volatile("7:\n\t" CORE_LOAD CORE_MMA("0", "", "0") CORE_FOLD_SLICE CORE_TAIL("0", CORE_AGAIN) : CORE_OUT : CORE_IN : "vcc", "scc", "memory");
    return bad;
}
__device__ __forceinline__ int core_st(rsrc_t r, unsigned so, const unsigned (&o)[4], const v8u (&W)[8][4], int idx, const LAS unsigned char* dst,
                                       v4u sent, unsigned cv, unsigned cs, v2u fval, unsigned fv, rsrc_t ru, const v4u (&u)[4], unsigned uv, unsigned us) {
    int bad = 0; v32u t0; v16u t1; v8u t2;
    asm volatile(CORE_LOAD
                 CORE_MMA("0",
                 "buffer_store_dwordx4 %[sent], %[cv], %[r], %[cs] offen sc1\n\t"
                 "buffer_store_dwordx2 %[fval], %[fv], %[r], 0 offen sc1\n\t"
                 "buffer_store_dwordx4 %[u0], %[uv], %[ru], %[us] offen sc1\n\t"
                 "buffer_store_dwordx4 %[u1], %[uv], %[ru], %[us] offen offset:1024 sc1\n\t"
                 "buffer_store_dwordx4 %[u2], %[uv], %[ru], %[us] offen offset:2048 sc1\n\t"
                 "buffer_store_dwordx4 %[u3], %[uv], %[ru], %[us] offen offset:3072 sc1\n\t", "6")
                 CORE_FOLD_SLICE CORE_TAIL("6", "")
                 : CORE_OUT : CORE_IN, [sent] "v"(sent), [cv] "v"(cv), [cs] "s"(cs), [fval] "v"(fval), [fv] "v"(fv), [ru] "s"(ru),
                   [u0] "v"(u[0]), [u1] "v"(u[1]), [u2] "v"(u[2]), [u3] "v"(u[3]), [uv] "v"(uv), [us] "s"(us) : "vcc", "scc", "memory");
    return bad;
}
#define UXW(k) "ds_write_b128 %[ud], %[x" #k "] offset:" #k "*1024\n\t"
#define UXL(k, s) "buffer_load_dwordx4 %[x" #k "], %[uv], %[ru], " s " offen offset:(" #k "&3)*1024 sc1\n\t"
__device__ __forceinline__ int core_ux(rsrc_t r, unsigned so, const unsigned (&o)[4], const v8u (&W)[8][4], int idx, const LAS unsigned char* dst,
                                       v4u (&x)[8], rsrc_t ru, unsigned uv, unsigned us0, unsigned us1, const LAS unsigned char* ud) {
    int bad = 0; v32u t0; v16u t1; v8u t2;
    asm volatile(CORE_LOAD "s_waitcnt vmcnt(8)\n\t" UXW(0) UXW(1) UXW(2) UXW(3) UXW(4) UXW(5) UXW(6) UXW(7)
                 CORE_MMA("0", "s_waitcnt lgkmcnt(0)\n\t" UXL(0, "%[us0]") UXL(1, "%[us0]") UXL(2, "%[us0]") UXL(3, "%[us0]") UXL(4, "%[us1]") UXL(5, "%[us1]") UXL(6, "%[us1]") UXL(7, "%[us1]"), "8")
                 CORE_FOLD_SLICE CORE_TAIL("8", "")
                 : CORE_OUT, [x0] "+v"(x[0]), [x1] "+v"(x[1]), [x2] "+v"(x[2]), [x3] "+v"(x[3]), [x4] "+v"(x[4]), [x5] "+v"(x[5]), [x6] "+v"(x[6]), [x7] "+v"(x[7])
                 : CORE_IN, [ru] "s"(ru), [uv] "v"(uv), [us0] "s"(us0), [us1] "s"(us1), [ud] "v"(ud) : "vcc", "scc", "memory");
    return bad;
}
template <int N> __device__ __forceinline__ bool has_sent(const v4u (&m)[N]) {
    unsigned x = 0;
#pragma unroll
    for (int f = 0; f < N; ++f) x = max(x, max(max(m[f].x, m[f].y), max(m[f].z, m[f].w)));
    return __builtin_amdgcn_ballot_w64(x == SENT) != 0;
}
__device__ __forceinline__ unsigned pair_of(unsigned cfg, unsigned odd) { return (cfg ^ (odd * 2)) * 8192u; }

template <int wave>
__device__ __forceinline__ void head_wave(const Args& a, LAS unsigned char* L, int head, int lane, int rep DGARG) {
    const int g = lane >> 4, n = lane & 15;
    const rsrc_t rc = mkrsrc(a.ws + O_COMM, COMM_BYTES);
    v8u W[8][4];
    load_w4s<3>(mkrsrc(wave < 2 ? a.wq : a.wk, DM * 4096 * 2), (unsigned)((32 * (g & 1) + 2 * (g >> 1)) * 4096 + head * HD + 64 * (wave & 1) + 4 * n) * 2, 2 * wave, W);
    const int p = (n >> 1) & 1, i = 2 * (n >> 2) + (n & 1);
    const int idx = p ? 0xEEEE : 0x4444;
    const unsigned mvoff = g * 256 + (i >> 2) * 128 + p * 64 + (i & 3) * 16;
    const unsigned mo[4] = {mvoff + ((wave + 0) & 3) * 2048, mvoff + ((wave + 1) & 3) * 2048, mvoff + ((wave + 2) & 3) * 2048, mvoff + ((wave + 3) & 3) * 2048};
    constexpr int QS = 288;
    DGT(1);
    if (!wait_init(rc, init_ld(rc))) return;
    if (!wait_flags(rc, lane < 4 ? C_KXF + (2 * head + (lane >> 1)) * 128 + (lane & 1) * 4 : C_CFG, 1)) return;
    DGT(2);
    const unsigned ck0 = ck();
    unsigned long long miss = 0;
    const unsigned nb = __builtin_amdgcn_readfirstlane(__builtin_amdgcn_raw_buffer_load_b32(rc, C_CFG, 0, 16)) - 1;
    const unsigned pa = pair_of(nb, 0), pb = pair_of(nb, 1), pp = pair_of(nb, rep >> 4);
    const unsigned fo = C_MET + pp + (rep & 15) * 128;
    const rsrc_t rk = mkrsrc(a.ws + O_KX, 16u << 20), rkm = mkrsrc(a.ws + O_KM, 4u << 20);
    constexpr int KXO = 8192;
    v4u kn[4];
    auto kx_get = [&](int sn) {
#pragma unroll
        for (int q = 0; q < 4; ++q) kn[q] = ld128c(rk, lane * 16 + q * 1024, (sn * NH + head) * 8192 + (wave & 1) * 4096);
    };
    auto kx_put = [&]() {
#pragma unroll
        for (int q = 0; q < 4; ++q) *(LAS v4u*)(L + KXO + (wave & 1) * 4096 + q * 1024 + lane * 16) = kn[q];
    };
    if (wave >= 2) { kx_get(0); kx_put(); }
    v2u word = {0u, 0u};
    LAS unsigned* tw = (LAS unsigned*)(L + 6144);
    if (wave == 3 && lane < 8) tw[lane] = 0;
    const float csc = 0.08838834764831845f * 1.4426950408889634f;
    const LAS unsigned char* wr = (L + (wave < 2 ? 0 : 8 * QS) + 2 * g * QS + 128 * (wave & 1) +
                                       (wave < 2 ? 32 * (n >> 3) + 16 * (n & 1) + 8 * ((n >> 2) & 1) + 4 * ((n >> 1) & 1) : 4 * n));
    const unsigned pxv = (4 + (head >> 4)) * BLK + (head & 15) * 512 + wave * 256 + (n >> 2) * 64 + g * 16 + (n & 3) * 4;
    const unsigned pmv = !(n & 1) ? 12 * BLK + (n < 8 ? pb : pa) + head * 128 + wave * 64 + g * 16 + (n & 7) * 2 : OOB;
    const unsigned clx = (4 + (head >> 4)) * BLK + (lane < 32 ? pa : pb) + (head & 15) * 512 + (lane & 31) * 16;
    const unsigned clm = lane < 16 ? 12 * BLK + (lane < 8 ? pa : pb) + head * 128 + (lane & 7) * 16 : OOB;
    const LAS unsigned char* qa = L + i * QS + 32 * g + 16 * p;
    const LAS unsigned char* ka = L + 8 * QS + (n & 7) * QS + 16 * g;
    int s = 0;
    asm volatile("" : "+s"(s));
    for (; s < NSEG; ++s) {
        const int slot = s & 3;
        unsigned so0 = s ? slot * BLK + pp : C_MSG0;
        unsigned psa = 2 * slot * BLK + pa, psb = 2 * slot * BLK + pb, psm = (slot >> 1) * BLK + (slot & 1) * 4096;
        v4u kx[8];
        f4 s0 = {0, 0, 0, 0}, s1 = s0, sm = s0;
        STDECL;
        unsigned tstart = 0, took = 0;
        BAR();
        if (wave == 3) {
            const v4u t0 = *(const LAS v4u*)tw, t1 = *(const LAS v4u*)(tw + 4);
            const int d0 = max((int)(t0.x - t0.z), (int)(t0.y - t0.z)) >> 8;
            took = (t0.z == 0 || (t0.w | t1.x | t1.y | t1.z)) ? 0 : d0;
        }
        if (wave < 2) {
#pragma unroll
            for (int q = 0; q < 8; ++q) { kx[q] = *(const LAS v4u*)(L + KXO + q * 1024 + lane * 16); asm volatile("" : "+v"(kx[q])); }
            asm volatile("" : "+v"(s0), "+v"(s1), "+v"(sm));
        }
        asm volatile("" : "+s"(so0), "+s"(psa), "+s"(psb), "+s"(psm));
        if (wave == 2 && s > 0) {
            const unsigned w = wait_word(rc, fo, s, word).x;
            if (!w) break;
            tstart = w & ~255u;
            STAMP(2);
            wait_until(w);
            STAMPR(3);
        }
        BAR();
        STAMP(0);
        const int rep_ = core<true>(rc, so0, mo, W, idx, wr);
        BAR();
        STAMP(1);
        if (wave < 2) {
            const v4u q0 = *(const LAS v4u*)qa, q1 = *(const LAS v4u*)(qa + 128);
            SCHED();
            const v4u k00 = *(const LAS v4u*)ka, k01 = *(const LAS v4u*)(ka + 64), k10 = *(const LAS v4u*)(ka + 128), k11 = *(const LAS v4u*)(ka + 192);
            SCHED();
            s0 = smfma(q0, kx[0], kx[1], s0, idx); s1 = smfma(q0, kx[4], kx[5], s1, idx);
            SCHED();
            s0 = smfma(q1, kx[2], kx[3], s0, idx); sm = smfma(q0, k00, k01, sm, idx);
            SCHED();
            s1 = smfma(q1, kx[6], kx[7], s1, idx); sm = smfma(q1, k10, k11, sm, idx);
            SCHED();
            constexpr int c = wave & 1;
            float a0 = s0[c] + s0[c + 2], a1 = s1[c] + s1[c + 2], amm = sm[c] + sm[c + 2];
            unsigned px, pm; float t0, t1;
            asm volatile(
                "v_max3_f32 %[t0], %[a0], %[a1], %[am]\n\t"
                "s_nop 1\n\tv_max_f32_dpp %[t0], %[t0], %[t0] row_ror:8 row_mask:0xf bank_mask:0xf\n\t"
                "s_nop 1\n\tv_max_f32_dpp %[t0], %[t0], %[t0] row_ror:4 row_mask:0xf bank_mask:0xf\n\t"
                "s_nop 1\n\tv_max_f32_dpp %[t0], %[t0], %[t0] row_ror:2 row_mask:0xf bank_mask:0xf\n\t"
                "s_nop 1\n\tv_max_f32_dpp %[t0], %[t0], %[t0] row_ror:1 row_mask:0xf bank_mask:0xf\n\t"
                "v_mul_f32 %[t0], %[c], %[t0]\n\t"
                "v_fma_f32 %[a0], %[a0], %[c], -%[t0]\n\t"
                "v_fma_f32 %[a1], %[a1], %[c], -%[t0]\n\t"
                "v_fma_f32 %[am], %[am], %[c], -%[t0]\n\t"
                "v_exp_f32 %[a0], %[a0]\n\t"
                "v_exp_f32 %[a1], %[a1]\n\t"
                "v_exp_f32 %[am], %[am]\n\t"
                "v_add_f32 %[t0], %[a0], %[a1]\n\t"
                "v_fmac_f32 %[t0], 0.5, %[am]\n\t"
                "s_nop 0\n\t"
                "v_mov_b32_dpp %[t1], %[am] quad_perm:[1,0,3,2] row_mask:0xf bank_mask:0xf\n\t"
                "v_add_f32_dpp %[t0], %[t0], %[t0] row_ror:8 row_mask:0xf bank_mask:0xf\n\t"
                "s_nop 1\n\tv_add_f32_dpp %[t0], %[t0], %[t0] row_ror:4 row_mask:0xf bank_mask:0xf\n\t"
                "s_nop 1\n\tv_add_f32_dpp %[t0], %[t0], %[t0] row_ror:2 row_mask:0xf bank_mask:0xf\n\t"
                "s_nop 1\n\tv_add_f32_dpp %[t0], %[t0], %[t0] row_ror:1 row_mask:0xf bank_mask:0xf\n\t"
                "v_rcp_f32 %[t0], %[t0]\n\t"
                "s_nop 0\n\t"
                "v_mul_f32 %[a0], %[a0], %[t0]\n\t"
                "v_mul_f32 %[a1], %[a1], %[t0]\n\t"
                "v_mul_f32 %[am], %[am], %[t0]\n\t"
                "v_cvt_pk_bf16_f32 %[px], |%[a0]|, |%[a1]|\n\t"
                "v_mul_f32 %[t1], %[t1], %[t0]\n\t"
                "v_cvt_pk_bf16_f32 %[pm], |%[am]|, |%[t1]|\n\t"
                : [a0] "+v"(a0), [a1] "+v"(a1), [am] "+v"(amm), [t0] "=&v"(t0), [t1] "=&v"(t1), [px] "=&v"(px), [pm] "=&v"(pm) : [c] "s"(csc));
            st32c(px, rc, pxv, psb); SCHED(); st32c(px, rc, pxv, psa); SCHED(); st32c(pm, rc, pmv, psm);
            SCHED();
            tw[wave] = now24();
            STAMP(2); STAMPR(3);
        } else if (wave == 2) {
            tw[2] = tstart;
            kx_get(min(s + 1, NSEG - 2));
            word = ld_word(rc, fo);
            kx_put();
        } else {
            const LAS unsigned char* ks = L + 8 * QS + g * QS + n * 16;
            const v4u k0 = *(const LAS v4u*)ks, k1 = *(const LAS v4u*)(ks + 4 * QS);
            DRAIN();
            kx_get(min(s + 1, NSEG - 2));
            FENCE();
            pause16<3>();
            st128c((v4u){SENT, SENT, SENT, SENT}, rc, clx, 2 * (slot ^ 2) * BLK);
            st128c((v4u){SENT, SENT, SENT, SENT}, rc, clm, psm ^ BLK);
            st64c((v2u){(unsigned)s, took}, rc, lane == 0 ? C_KMF + head * 128 : OOB, 0);
            st128c(k0, rkm, lane * 16, (s * NH + head) * 2048); st128c(k1, rkm, lane * 16 + 1024, (s * NH + head) * 2048);
            kx_put();
            if (s == NSEG - 1) { DRAIN(); st32c(s + 1, rc, lane == 0 ? C_KMF + head * 128 : OOB, 0); }
        }
        tw[3 + wave] = rep_;
        STFLUSH;
        if (rep_) miss |= 1ull << s;
        if (rep_ >= 0x20000) break;
    }
    STDUMP; DGT(3); DG(4, miss); DG(5, miss >> 32); DG(13, ck() - ck0);
    DRAIN();
}

template <int wave>
__device__ __forceinline__ void slice_wave(const Args& a, LAS unsigned char* L, int sl, int lane, int rep DGARG) {
    const int g = lane >> 4, n = lane & 15;
    const rsrc_t rc = mkrsrc(a.ws + O_COMM, COMM_BYTES);
    v8u W[8][4];
    {
        const rsrc_t rb = mkrsrc(a.ws + O_BS + (size_t)sl * 262144 + wave * 65536, 65536);
        const unsigned bv = (n >> 3) * 1024 + g * 128 + (n & 7) * 16;
#pragma unroll
        for (int r = 0; r < 2; ++r) {
            const int line = r ? 16 * wave + (n | 1) : (lane & 16) ? 64 + 16 * wave + n : 16 * wave + (n & ~1);
            if (!wait_flags<BNAP>(rc, C_BF + line * 128 + (sl >> 4) * 4, 1)) return;
            DGT(8 + r);
#pragma unroll
            for (int k = 0; k < 8; ++k) {
                const int f = (k + 2 * wave) & 7;
                if ((f >> 2) != r) continue;
#pragma unroll
                for (int t = 0; t < 4; ++t) {
                    const unsigned so = f * 8192 + (t >> 1) * 4096;
                    W[k][t] = __builtin_bit_cast(v8u, cat(ld128b(rb, bv + (t & 1) * 2048, so), ld128b(rb, bv + (t & 1) * 2048 + 512, so)));
                }
            }
        }
#pragma unroll
        for (int k = 0; k < 8; ++k)
#pragma unroll
            for (int t = 0; t < 4; ++t) asm volatile("" : "+a"(W[k][t]));
    }
    const int p = (n >> 1) & 1, i = 2 * (n >> 2) + (n & 1);
    const int idx = p ? 0xEEEE : 0x4444;
    const unsigned mvoff = g * 256 + (i >> 2) * 128 + p * 64 + (i & 3) * 16;
    const unsigned mo[4] = {mvoff + ((wave + 0) & 3) * 2048, mvoff + ((wave + 1) & 3) * 2048, mvoff + ((wave + 2) & 3) * 2048, mvoff + ((wave + 3) & 3) * 2048};
    constexpr int UXO = 8192;
    DGT(1);
    if (!wait_flags<8>(rc, lane < 32 && wave >= 2 ? C_VOF + lane * 128 : C_CFG, lane < 32 && wave >= 2 ? 2 : 1)) return;
    DGT(2);
    unsigned long long miss = 0, missp = 0;
    const unsigned nb = __builtin_amdgcn_readfirstlane(__builtin_amdgcn_raw_buffer_load_b32(rc, C_CFG, 0, 16)) - 1;
    const unsigned pa = pair_of(nb, 0), pp = pair_of(nb, rep >> 4);
    const unsigned fo = C_MET + pp + (rep & 15) * 128;
    const rsrc_t rv = mkrsrc(a.ws + O_VO, 64u << 20), rum = mkrsrc(a.ws + O_UM, 16u << 20);
    const unsigned uvoff = p * 512 + g * 128 + i * 16;
    v4u un[8];
    auto ux_load = [&](int sn) {
#pragma unroll
        for (int F = 0; F < 8; ++F) un[F] = ld128c(rv, lane * 16 + (F & 3) * 1024, (sn * 64 + sl) * 16384 + (wave & 1) * 8192 + (F >> 2) * 4096);
    };
    if (wave >= 2) {
        ux_load(0);
        asm volatile("" : "+v"(un[0]), "+v"(un[1]), "+v"(un[2]), "+v"(un[3]), "+v"(un[4]), "+v"(un[5]), "+v"(un[6]), "+v"(un[7]));
    }
    const int pblk = n >> 3, ppar = (n >> 2) & 1, pg = n & 3, orow = 2 * pg + ppar;
    const unsigned pxoff = pblk * 512 + ppar * 256 + g * 64 + pg * 16, pmoff = (4 * pblk + g) * 128 + ppar * 64 + pg * 16;
    const unsigned mbase = (sl >> 1) * 256 + (sl & 1) * 64, pb = pair_of(nb, 1);
    const unsigned mst = (n < 8 ? pa : pb) + mbase + (orow >> 2) * 128 + (orow & 3) * 16 + g * 4;
    const unsigned clv = lane < 16 ? (lane < 8 ? pa : pb) + mbase + ((lane >> 2) & 1) * 128 + (lane & 3) * 16 : OOB;
    const unsigned long long hi8 = 0xff00ff00ff00ff00ull;
    const LAS unsigned char* uw = L + 8 * wave * 128 + n * 16 + g * 4;
    v4u u[4] = {};
    unsigned tp = 0, took = 0;
    LAS unsigned* tw = (LAS unsigned*)(L + 6144);
    if (wave == 1 && lane == 0) tw[0] = 0;
    int s = 0;
    asm volatile("" : "+s"(s));
    for (; s < NSEG; ++s) {
        const int slot = s & 3;
        const unsigned so0 = s ? slot * BLK + pp : C_MSG0;
        bool fail = false;
        const bool last = s == NSEG - 1;
        STDECL; STAMP(0);
        const unsigned uso = (min(s + 1, NSEG - 2) * 64 + sl) * 16384 + (wave & 1) * 8192;
        const int bad = wave == 0 ? core<false>(rc, so0, mo, W, idx, uw)
                      : wave == 1 ? core_st(rc, so0, mo, W, idx, uw, (v4u){SENT, SENT, SENT, SENT}, clv, ((s + 2) & 3) * BLK, (v2u){(unsigned)max(s - 1, 0), took}, lane == 0 ? C_UMF + sl * 128 : OOB,
                                            rum, u, s ? lane * 16 : OOB, (max(s - 1, 0) * 64 + sl) * 4096)
                                  : core_ux(rc, so0, mo, W, idx, uw, un, rv, lane * 16, uso, uso + 4096, L + UXO + (wave & 1) * 8192 + lane * 16);
        if (__builtin_expect(bad, 0)) {
            miss |= 1ull << s;
            fail = (wave ? core<false>(rc, so0, mo, W, idx, uw) : bad) >= 0x20000;
        }
        if (wave != 0) STAMP(1);
        if (wave == 0) {
            if (last) { BAR(); BAR(); break; }
            if (__builtin_expect(s == 0 || bad || (int)(now24() - tp) > 60 * U, 0)) {
                if (s) { const v2u w = wait_word(rc, fo, s + 1, (v2u){0u, 0u}); tp = w.x - (w.y & 0xffffu) * U; } else tp = now24();
            }
            const unsigned pso = (4 + 2 * slot) * BLK + pp;
            unsigned q0 = pso, q1 = pso + 4096, q2 = pso + BLK, q3 = q2 + 4096, q4 = (12 + (slot >> 1)) * BLK + pp + (slot & 1) * 4096, nso = ((s + 1) & 3) * BLK, nanw = OOB;
            f4 d = {0, 0, 0, 0}, e = d;
            asm volatile("" : "+s"(q0), "+s"(q1), "+s"(q2), "+s"(q3), "+s"(q4), "+s"(nso), "+v"(d), "+v"(e), "+v"(nanw));
            v2u word = {0u, 0u};
            v4u pf[20];
            unsigned tend = 0;
            auto finish = [&](bool first) -> unsigned long long {
                v4u ux[16], um[4];
                FENCE();
#pragma unroll
                for (int F = 0; F < 16; ++F) { pf[F] = ld128c(rc, pxoff + (F & 3) * 1024, (F >> 2) == 0 ? q0 : (F >> 2) == 1 ? q1 : (F >> 2) == 2 ? q2 : q3); SCHED(); }
#pragma unroll
                for (int F = 0; F < 4; ++F) { pf[16 + F] = ld128c(rc, pmoff + F * 1024, q4); SCHED(); }
                const v2u wv = ld_word(rc, fo);
                SCHED();
                if (first) BAR();
#pragma unroll
                for (int F = 0; F < 16; ++F) ux[F] = *(const LAS v4u*)(L + UXO + uvoff + F * 1024);
#pragma unroll
                for (int F = 0; F < 4; ++F) um[F] = *(const LAS v4u*)(L + (8 * F + 4 * p + g) * 128 + i * 16);
                SCHED();
#pragma unroll
                for (int F = 0; F < 16; F += 2) { MMA_VV(d, ux[F], pf[F]); MMA_VV(e, ux[F + 1], pf[F + 1]); }
                MMA_VV(d, um[0], pf[16]); MMA_VV(e, um[1], pf[17]); MMA_VV(d, um[2], pf[18]); MMA_VV(e, um[3], pf[19]);
                unsigned outw, adr; unsigned long long un_;
                float r0, r1, t0, t1;
                asm volatile("s_nop 6\n\t"
                             "v_cndmask_b32 %[r0], %[d0], %[d2], %[m]\n\t"
                             "v_cndmask_b32 %[r1], %[d1], %[d3], %[m]\n\t"
                             "v_cndmask_b32 %[t0], %[e0], %[e2], %[m]\n\t"
                             "v_cndmask_b32 %[t1], %[e1], %[e3], %[m]\n\t"
                             "v_add_f32 %[r0], %[r0], %[t0]\n\t"
                             "v_add_f32 %[r1], %[r1], %[t1]\n\t"
                             "s_nop 0\n\t"
                             "v_add_f32_dpp %[r0], %[r0], %[r0] row_ror:8 row_mask:0xf bank_mask:0xf\n\t"
                             "v_add_f32_dpp %[r1], %[r1], %[r1] row_ror:8 row_mask:0xf bank_mask:0xf\n\t"
                             "v_cmp_u_f32 %[u], %[r0], %[r1]\n\t"
                             "v_cvt_pk_bf16_f32 %[o], %[r0], %[r1]\n\t"
                             "v_min_u32 %[o], -2, %[o]\n\t"
                             "v_cndmask_b32 %[ad], %[ms], %[nw], %[u]"
                             : [r0] "=&v"(r0), [r1] "=&v"(r1), [t0] "=&v"(t0), [t1] "=&v"(t1), [o] "=&v"(outw), [u] "=&s"(un_), [ad] "=&v"(adr)
                             : [d0] "v"(d.x), [d1] "v"(d.y), [d2] "v"(d.z), [d3] "v"(d.w), [e0] "v"(e.x), [e1] "v"(e.y), [e2] "v"(e.z), [e3] "v"(e.w), [nw] "v"(nanw), [ms] "v"(mst), [m] "s"(hi8));
                st32c(outw, rc, adr, nso);
                SCHED();
                tend = now24();
                word = rfl2(wv);
                asm volatile("" : "+s"(un_), "+s"(word) :: "memory");
                return un_;
            };
            wait_until(tp);
            STAMP(1);
            unsigned long long r = finish(true);
            tw[0] = (r || !s) ? 0 : (int)(tend - (tp & ~255u)) >> 8;
            if (__builtin_expect(r != 0, 0)) {
                missp |= 1ull << s;
                for (int att = 0; !fail; ++att) {
                    const bool genuine = att > 0 && !has_sent<20>(pf);
                    if (genuine) nanw = mst;
                    asm volatile("s_nop 7" : "+v"(d), "+v"(e));
                    d = (f4){0, 0, 0, 0}; e = d;
                    asm volatile("s_nop 1" : "+v"(d), "+v"(e));
                    if (att == 0 && s > 0) { const v2u w = wait_word(rc, fo, s + 1, word); wait_until(w.x - (w.y & 0xffffu) * U); }
                    r = finish(false);
                    if (r == 0 || genuine) break;
                    fail = att > SPIN;
                }
            }
            STAMP(2); STAMPR(3);
            const v2u w = wait_word(rc, fo, s + 1, word);
            tp = w.x + (w.y >> 16) * U;
            if (!w.x) fail = true;
            STFLUSH;
        } else {
            if (wave == 1) asm volatile("s_waitcnt vmcnt(5)" ::: "memory");
            STAMP(2);
            BAR();
            STFLUSH;
            if (wave == 1) {
#pragma unroll
                for (int q = 0; q < 4; ++q) u[q] = *(const LAS v4u*)(L + q * 1024 + lane * 16);
                if (last) {
#pragma unroll
                    for (int q = 0; q < 4; ++q) st128c(u[q], rum, lane * 16 + q * 1024, (s * 64 + sl) * 4096);
                }
                DRAIN();
                if (last) st32c(s + 1, rc, lane == 0 ? C_UMF + sl * 128 : OOB, 0);
                took = tw[0];
                const unsigned w = last ? 1 : wait_word(rc, fo, s + 1, (v2u){0u, 0u}).x;
                if (!w) fail = true;
                if (!last) wait_until(w + a.dms * U);
            }
        }
        if (fail) break;
        BAR();
    }
    STDUMP; DGT(3); DG(4, miss); DG(5, miss >> 32); DG(6, missp); DG(7, missp >> 32);
    DRAIN();
}

__device__ __forceinline__ void probe_pages(const Args& a, int lane DGARG) {
    constexpr int wave = 0;
    const rsrc_t rc = mkrsrc(a.ws + O_COMM, COMM_BYTES);
#pragma unroll
    for (int k = 0; k < 2; ++k) {
        const int ln = k * 8 + (lane >> 3);
        st128c((v4u){0u, 0u, 0u, 0u}, rc, ln < 12 ? C_PROBE + (ln & 3) * 8192 + ((ln >> 2) & 1) * 4096 + (ln >> 2) * 384 + (lane & 7) * 16 : OOB, 0);
    }
    DRAIN();
    unsigned best[4] = {0xffffffffu, 0xffffffffu, 0xffffffffu, 0xffffffffu};
    for (int r = 0; r < 3; ++r) {
#pragma unroll
        for (int k = 0; k < 4; ++k) {
            const GAS unsigned* q = (const GAS unsigned*)(a.ws + O_COMM + C_PROBE + k * 8192 + (r & 1) * 4096 + r * 384);
            unsigned v;
            const unsigned c1 = ck();
            asm volatile("s_waitcnt lgkmcnt(0)\n\tglobal_load_dword %0, %1, off sc1\n\ts_waitcnt vmcnt(0)" : "=v"(v) : "v"(q) : "memory");
            const unsigned c2 = ck();
            best[k] = min(best[k], c2 - c1);
        }
    }
    const unsigned n01 = best[1] < best[0] ? 1u : 0u, n23 = best[3] < best[2] ? 3u : 2u;
    const unsigned nb = min(best[2], best[3]) < min(best[0], best[1]) ? n23 : n01;
    DGT(30); DG(28, best[0] | (best[1] << 16)); DG(29, best[2] | (best[3] << 16));
    wait_init(rc, init_ld(rc));
    st32c(nb + 1, rc, lane == 0 ? C_NB : OOB, 0);
}

__device__ __forceinline__ void open_rings(const Args& a, int tid, int lane) {
    const rsrc_t rc = mkrsrc(a.ws + O_COMM, COMM_BYTES);
#pragma unroll
    for (int k = 0; k < 2; ++k) {
        const unsigned o = (k * 256 + tid) * 16, f = o >> 10, G = (o >> 8) & 3, ih = (o >> 7) & 1, p = (o >> 6) & 1, i3 = (o >> 4) & 3;
        const GAS u16* src = (const GAS u16*)a.mem0 + (4 * ih + i3) * DM + 64 * f + 2 * G + p;
        v4u v;
#pragma unroll
        for (int q = 0; q < 4; ++q) v[q] = min((unsigned)src[16 * q] | ((unsigned)src[16 * q + 8] << 16), SENT - 1);
        st128c(v, rc, C_MSG0 + o, 0);
    }
    wait_flags(rc, C_NB, 1);
    const unsigned nb1 = __builtin_amdgcn_readfirstlane(__builtin_amdgcn_raw_buffer_load_b32(rc, C_NB, 0, 16));
    const v4u sent = {SENT, SENT, SENT, SENT}, z = {0u, 0u, 0u, 0u};
#pragma unroll
    for (int c = 0; c < 2; ++c) {
        const unsigned pr = pair_of((nb1 - 1) & 3, c);
#pragma unroll
        for (int k = 0; k < 12; ++k) {
            const int blk = k < 2 ? 1 : k < 10 ? 3 + (k >> 1) : 12;
            st128r(sent, rc, tid * 16 + (k & 1) * 4096, blk * BLK + pr);
        }
        st128r(z, rc, tid < 128 ? tid * 16 : OOB, C_MET + pr);
    }
    DRAIN();
    BAR();
    st32c(nb1, rc, tid == 0 ? C_CFG : OOB, 0);
}

__device__ __forceinline__ void metronome(const Args& a, int lane DGARG) {
    const rsrc_t rc = mkrsrc(a.ws + O_COMM, COMM_BYTES);
    constexpr int wave = 0; unsigned long long miss = 0, late = 0;
    if (!wait_flags(rc, C_CFG, 1)) return;
    const unsigned nb = __builtin_amdgcn_readfirstlane(__builtin_amdgcn_raw_buffer_load_b32(rc, C_CFG, 0, 16)) - 1;
    const unsigned pa = pair_of(nb, 0);
    const unsigned fst = lane < 32 ? C_MET + pair_of(nb, lane >> 4) + (lane & 15) * 128 : OOB;
    DGT(2);
    unsigned rdy = 0;
    bool shaky = true;
    for (int att = 0;; ++att) {
        FENCE();
        const unsigned x = __builtin_amdgcn_raw_buffer_load_b32(rc, lane * 64, BLK + pa, 16), y = __builtin_amdgcn_raw_buffer_load_b32(rc, lane * 64, BLK + pa + 4096, 16);
        const unsigned r = __builtin_amdgcn_raw_buffer_load_b32(rc, C_RDY, 0, 16);
        if (__builtin_amdgcn_ballot_w64(max(x, y) == SENT || r < 3) == 0) break;
        if (att > SPIN * 4) return;
    }
    unsigned tr = now24() + a.guard * U;
    int da = a.da, dm = a.period - a.da, eh = (da - a.gh) * 16, es = (dm - a.gs) * 16;
    da += a.send;
    st64c((v2u){tr | 1, (unsigned)(da << 16 | dm)}, rc, fst, 0);
    DGT(64);
    shaky = false;
    for (int s = 1; s < NSEG; ++s) {
        const int slot = s & 3;
        const unsigned so0 = s ? slot * BLK + pa : C_MSG0;
        const unsigned need = min(s + 3, NSEG - 1);
        bool hit = false;
        wait_until(tr);
        for (int att = 0;; ++att) {
            FENCE();
            const unsigned x = __builtin_amdgcn_raw_buffer_load_b32(rc, lane * 64, so0, 16), y = __builtin_amdgcn_raw_buffer_load_b32(rc, lane * 64, so0 + 4096, 16);
            if (__builtin_expect(__builtin_amdgcn_ballot_w64(max(x, y) == SENT) == 0, 1)) break;
            if (att > SPIN) return;
            tr = now24() + (shaky ? a.du : a.db) * U; miss |= 1ull << s; hit = true;
        }
        shaky = hit;
        dm = (es >> 4) + a.gs + (s < 3 ? a.send >> (s - 1) : 0);
        unsigned tn = tr + (da + dm) * U;
        da = (eh >> 4) + a.gh + (s < 2 ? a.send >> 1 : 0);
        if (__builtin_expect(rdy < need, 0)) {
            if (!wait_flags(rc, C_RDY, need)) return;
            const unsigned t2 = now24() + a.guard * U;
            if ((int)(t2 - tn) > 0) { tn = t2; late |= 1ull << s; }
        }
        st64c((v2u){tn | (s + 1), (unsigned)(da << 16 | dm)}, rc, fst, 0);
        DG(32 + (s >> 1), da << 16 | dm);
        DGT(64 + s);
        wait_until(tn - 200 * U);
        const v2u rp = rfl2(ld_word(rc, C_RDY));
        rdy = rp.x;
        if (s > 3) {
            const int rh = (int)(rp.y >> 16) * 16, rs = (int)(rp.y & 0xffffu) * 16;
            if (rh) eh += rh > eh ? min(rh - eh, 4 * a.decay) : -a.decay;
            if (rs) es += rs > es ? min(rs - es, 4 * a.decay) : -a.decay;
            if (hit) { eh = min(eh + 16, 500 * 16); es = min(es + 16, 400 * 16); }
        }
        tr = tn;
    }
    {
        const v2u k = ld_word(rc, C_INIT + (lane & 31) * 128 + 8);
        st64c(k, rc, lane < 32 ? C_INIT + lane * 128 : OOB, 0);
    }
    DGT(3); DG(4, miss); DG(5, miss >> 32); DG(6, late); DG(7, late >> 32);
    DRAIN();
}

__device__ __forceinline__ void gatekeeper(const Args& a, int lane) {
    const rsrc_t rc = mkrsrc(a.ws + O_COMM, COMM_BYTES);
    unsigned cur = 0, curt = 0;
    bool done = false;
    for (int i = 0; i < SPIN * 4 && !done; ++i) {
        FENCE();
        const v2u k = __builtin_amdgcn_raw_buffer_load_b64(rc, C_KXF + lane * 128, 0, 16);
        const unsigned v = __builtin_amdgcn_raw_buffer_load_b32(rc, C_VOF + (lane >> 1) * 128 + (lane & 1) * 4, 0, 16);
        const v2u th = ld_word(rc, C_KMF + (lane & 31) * 128), ts = ld_word(rc, C_UMF + lane * 128);
        unsigned r = min(2 * min(k.x, k.y) + (lane & 1), v);
        unsigned mh = min(th.y, 0xffffu), ms = min(ts.y, 0xffffu);
#pragma unroll
        for (int m = 32; m >= 1; m >>= 1) { r = min(r, (unsigned)__shfl_xor((int)r, m)); mh = max(mh, (unsigned)__shfl_xor((int)mh, m)); ms = max(ms, (unsigned)__shfl_xor((int)ms, m)); }
        r = __builtin_amdgcn_readfirstlane(r);
        const unsigned t = __builtin_amdgcn_readfirstlane(mh << 16 | ms);
        done = r >= NSEG && __builtin_amdgcn_readfirstlane(th.x) >= NSEG - 2;
        FENCE();
        if (r > cur || t != curt) { cur = r; curt = t; st64c((v2u){r, t}, rc, lane == 0 ? C_RDY : OOB, 0); }
        __builtin_amdgcn_s_sleep(4);
    }
    DRAIN();
}

__device__ __forceinline__ void prefetcher(const Args& a, int cls, int part, int nparts, int lane) {
    const rsrc_t rc = mkrsrc(a.ws + O_COMM, COMM_BYTES), rk = mkrsrc(a.ws + O_KX, 80u << 20);
    if (!wait_flags(rc, C_CFG, 1)) return;
    const unsigned nb = __builtin_amdgcn_readfirstlane(__builtin_amdgcn_raw_buffer_load_b32(rc, C_CFG, 0, 16)) - 1;
    const unsigned fo = C_MET + pair_of(nb, cls & 1) + ((cls >> 1) * 8 + (part & 7)) * 128;
    const unsigned vbase = cls * 16 * 16384u, nv = 32, nk = 8;
    for (int s = 0; s < NSEG - 3; ++s) {
        unsigned w = 0;
        for (int i = 0; (w & 255u) < s + 1; ++i) {
            if (i > SPIN * 4) return;
            FENCE();
            w = __builtin_amdgcn_readfirstlane(__builtin_amdgcn_raw_buffer_load_b32(rc, fo, 0, 16));
            __builtin_amdgcn_s_sleep(2);
        }
        unsigned x[4];
#pragma unroll
        for (int j = 0; j < 4; ++j) {
            const unsigned c = part + j * nparts;
            const unsigned so = c < nk ? (unsigned)((s + 2) * NH + cls * 8 + c) * 8192u : (16u << 20) + (unsigned)(s + 2) * 1048576u + vbase + (c - nk) * 8192;
            x[j] = __builtin_amdgcn_raw_buffer_load_b32(rk, c < nv + nk ? lane * 128 : OOB, so, 16);
        }
#pragma unroll
        for (int j = 0; j < 4; ++j) asm volatile("" :: "v"(x[j]));
    }
}

template <class Hook>
__device__ __forceinline__ void proj_seg(const v4u (&W)[16][4], const LAS unsigned char* X, int lane, int rot, f4 (&acc)[4][2], Hook hook) {
    v4u x0[16], x1[16];
    const LAS unsigned char* Xh[2] = {X + rot * 8192 + lane * 16, X + (rot ^ 1) * 8192 + lane * 16};
#pragma unroll
    for (int ks = 0; ks < 2; ++ks) { x0[ks] = *(const LAS v4u*)(Xh[0] + ks * 1024); x1[ks] = *(const LAS v4u*)(Xh[0] + 16384 + ks * 1024); }
#pragma unroll
    for (int k = 0; k < 16; ++k) {
#pragma unroll
        for (int t = 0; t < 4; ++t) {
            SCHED();
            if (k == 0) { MMA_WA0(acc[t][0], W[k][t], x0[k]); MMA_WA0(acc[t][1], W[k][t], x1[k]); }
            else { MMA_WA(acc[t][0], W[k][t], x0[k]); MMA_WA(acc[t][1], W[k][t], x1[k]); }
            SCHED();
            if (t == 0 && k + 2 < 16) { x0[k + 2] = *(const LAS v4u*)(Xh[(k + 2) >> 3] + ((k + 2) & 7) * 1024); x1[k + 2] = *(const LAS v4u*)(Xh[(k + 2) >> 3] + 16384 + ((k + 2) & 7) * 1024); }
            if (t == 1) hook(k, 0);
            if (t == 2) hook(k, 1);
        }
    }
    SCHED();
    asm volatile("s_nop 7" : "+v"(acc[0][0]), "+v"(acc[0][1]), "+v"(acc[1][0]), "+v"(acc[1][1]), "+v"(acc[2][0]), "+v"(acc[2][1]), "+v"(acc[3][0]), "+v"(acc[3][1]));
}
__device__ __forceinline__ v4u pack2(const f4& lo, const f4& hi) { return (v4u){pk(lo[0], hi[0]), pk(lo[1], hi[1]), pk(lo[2], hi[2]), pk(lo[3], hi[3])}; }
struct NatRow { int rot; __device__ int operator()(int ks, int jj) const { return 32 * (ks ^ (8 * rot)) + jj; } };
struct PairRow { __device__ constexpr int operator()(int ks, int jj) const { return 64 * (ks >> 1) + 2 * (ks & 1) + 4 * (jj >> 1) + (jj & 1); } };

__device__ __forceinline__ void kq_role(const Args& a, LAS unsigned char* L, int id, int wave, int lane DGARG) {
    const int h = id >> 1, par = id & 1, kr = ROTW ? par : 0, g = lane >> 4, n = lane & 15;
    const rsrc_t rc = mkrsrc(a.ws + O_COMM, COMM_BYTES), rx = mkrsrc(a.ws + O_XN, 2u << 20), rk = mkrsrc(a.ws + O_KX, 16u << 20), rkm = mkrsrc(a.ws + O_KM, 4u << 20),
                 rpx = mkrsrc(a.ws + O_PX, 4u << 20), rpm = mkrsrc(a.ws + O_PM, 1u << 20);
    const rsrc_t rw = mkrsrc(a.ws + O_VO, (unsigned)(O_END - O_VO));
    unsigned warmed = 0, wx[7] = {};
    const unsigned kv = wave < 2 ? lane * 16 : OOB;
    constexpr int XO = 0, RO = 65536;
    v4u xs[8];
    auto x_load = [&](int j) {
#pragma unroll
        for (int q = 0; q < 8; ++q) xs[q] = ld128c(rx, lane * 16 + (q & 3) * 1024, (2 * j + par) * 32768 + wave * 8192 + (q >> 2) * 4096);
    };
    auto x_put = [&](int j) {
#pragma unroll
        for (int q = 0; q < 8; ++q) *(LAS v4u*)(L + XO + (j & 1) * 32768 + wave * 8192 + q * 1024 + lane * 16) = xs[q];
    };
    if (wave >= 2) {
        nap(a.dk);
        wait_init(rc, init_ld(rc));
        wait_xn(rc, 4 + (id >> 4), lane, false);
        v4u x0[16];
#pragma unroll
        for (int q = 0; q < 16; ++q) x0[q] = ld128c(rx, lane * 16, par * 32768 + (wave & 1) * 16384 + q * 1024);
#pragma unroll
        for (int q = 0; q < 16; ++q) *(LAS v4u*)(L + XO + (wave & 1) * 16384 + q * 1024 + lane * 16) = x0[q];
        BAR();
    }
    v4u W[16][4];
    load_w4<16, 7, true>(mkrsrc(wave < 2 ? a.wk : a.wq, DM * 4096 * 2), (unsigned)(8 * g * 4096 + h * HD + 64 * (wave & 1) + 4 * n) * 2, 8192, NatRow{kr}, W, [&](int stage) {
        if (stage == 15) { if (wave < 2) BAR(); x_load(1); }
    });
    DGT(1); DGT(8); DGT(9);
    for (int it = 0; it < 36; ++it) {
        if (it == 6) wait_xn(rc, 4 + (id >> 4), lane, true);
        unsigned seen = 0; bool early = false; v4u km[4];
        if (it < 32) {
            f4 acc[4][2];
            LAS unsigned char* xd = L + XO + ((it + 1) & 1) * 32768 + wave * 8192 + lane * 16;
            const unsigned xso = (2 * min(it + 2, 31) + par) * 32768 + wave * 8192;
            const unsigned ptf = (lane == 0 && wave >= 2 && it >= 5) ? C_PTF + id * 128 + (wave & 1) * 4 : OOB;
            proj_seg(W, L + XO + (it & 1) * 32768, lane, kr, acc, [&](int k, int half) {
                if (k < 8) { if (half == 0) *(LAS v4u*)(xd + k * 1024) = xs[k]; else xs[k] = ld128c(rx, lane * 16 + (k & 3) * 1024, xso + (k >> 2) * 4096); }
                if (k == 8 && half == 0) seen = __builtin_amdgcn_raw_buffer_load_b32(rc, wave < 2 ? C_OUTP + (id >> 4) * 128 : C_KMF + h * 128, 0, 16);
                if (k == 13 && half == 0) {
                    DRAIN(); st32c(it - 4, rc, ptf, 0);
                    seen = __builtin_amdgcn_readfirstlane(seen);
#pragma unroll
                    for (int q = 0; q < 7; ++q) asm volatile("" :: "v"(wx[q]));
                    FENCE();
                    early = wave >= 2 && it >= 4 && seen >= (unsigned)(2 * it - 7 + par);
#pragma unroll
                    for (int ks = 0; ks < 4; ++ks) km[ks] = ld128c(rkm, early ? (n & 7) * 256 + g * 16 + ks * 64 : OOB, (max(2 * it - 8 + par, 0) * NH + h) * 2048);
                }
            });
            LAS unsigned char* R = L + RO + (it % 5) * 16384 + (wave >> 1) * 8192 + (wave & 1) * 2048 + lane * 16;
#pragma unroll
            for (int tt = 0; tt < 2; ++tt)
#pragma unroll
                for (int y = 0; y < 2; ++y) {
                    const v4u c = pack2(acc[2 * y][tt], acc[2 * y + 1][tt]);
                    *(LAS v4u*)(R + tt * 4096 + y * 1024) = c;
                    st128c(c, rk, kv + y * 1024, ((2 * it + par) * NH + h) * 8192 + tt * 4096 + (wave & 1) * 2048);
                }
            if (wave < 2) {
                DRAIN(); st32c(it + 1, rc, lane == 0 ? C_KXF + id * 128 + wave * 4 : OOB, 0);
                if (seen != warmed) {
                    warmed = seen;
                    const unsigned part = (id & 15) * 2 + wave, sg = seen & 63, sp = max(sg, 4u) - 4;
#pragma unroll
                    for (int q = 0; q < 4; ++q) wx[q] = __builtin_amdgcn_raw_buffer_load_b32(rw, lane * 128, sg * 1048576u + part * 32768 + q * 8192, 16);
                    wx[4] = __builtin_amdgcn_raw_buffer_load_b32(rw, (seen & 256) && part < 14 ? lane * 128 : OOB, (unsigned)(O_HH - O_VO) + sg * 114688u + part * 8192, 16);
                    wx[5] = __builtin_amdgcn_raw_buffer_load_b32(rw, (seen & 512) ? lane * 128 : OOB, (unsigned)(O_UM - O_VO) + sp * 262144u + part * 8192, 16);
                    wx[6] = __builtin_amdgcn_raw_buffer_load_b32(rw, (seen & 512) && part < 8 ? lane * 128 : OOB, (unsigned)(O_PX - O_VO) + sp * 65536u + part * 8192, 16);
                }
            }
            DGT(64 + it);
        }
        if (it >= 4 && wave >= 2) {
            const int j = it - 4, s = 2 * j + par, tt = wave & 1;
            const LAS unsigned char* R = L + RO + (j % 5) * 16384 + lane * 16;
            f4 s0 = {0, 0, 0, 0}, s1 = s0, sm = s0;
            v4u q[4], k0[4], k1[4];
#pragma unroll
            for (int ks = 0; ks < 4; ++ks) { q[ks] = *(const LAS v4u*)(R + 8192 + tt * 4096 + ks * 1024); k0[ks] = *(const LAS v4u*)(R + ks * 1024); k1[ks] = *(const LAS v4u*)(R + 4096 + ks * 1024); }
            asm volatile("s_nop 1" : "+v"(s0), "+v"(s1), "+v"(sm));
            SCHED();
#pragma unroll
            for (int ks = 0; ks < 4; ++ks) { MMA_VV(s0, k0[ks], q[ks]); MMA_VV(s1, k1[ks], q[ks]); }
            asm volatile("s_nop 7" : "+v"(s0), "+v"(s1));
            const float sc = 0.08838834764831845f, l2e = 1.4426950408889634f;
            const int tok = 16 * tt + n;
            auto scale2 = [&](float x, float y, float& ox, float& oy) { const unsigned r = pk(x, y), t = pk(bflo(r) * sc, bfhi(r) * sc); ox = bflo(t); oy = bfhi(t); };
            float v0[4], v1[4], vm[4];
#pragma unroll
            for (int c = 0; c < 4; ++c) {
                scale2(s0[c], s1[c], v0[c], v1[c]);
                asm volatile("" : "+v"(v0[c]), "+v"(v1[c]));
                v0[c] = (4 * g + c <= tok) ? v0[c] : -1e9f;
                v1[c] = (16 + 4 * g + c <= tok) ? v1[c] : -1e9f;
            }
            float mx = fmaxf(fmaxf(fmaxf(v0[0], v0[1]), fmaxf(v0[2], v0[3])), fmaxf(fmaxf(v1[0], v1[1]), fmaxf(v1[2], v1[3])));
            if (!early) {
                if (!wait_flags<1>(rc, C_KMF + h * 128, s + 1)) return;
#pragma unroll
                for (int ks = 0; ks < 4; ++ks) km[ks] = ld128c(rkm, (n & 7) * 256 + g * 16 + ks * 64, (s * NH + h) * 2048);
                asm volatile("" : "+v"(km[0]), "+v"(km[1]), "+v"(km[2]), "+v"(km[3]));
            }
            asm volatile("s_nop 1");
#pragma unroll
            for (int ks = 0; ks < 4; ++ks) MMA_VV(sm, km[ks], q[ks]);
            asm volatile("s_nop 7" : "+v"(sm));
            scale2(sm[0], sm[1], vm[0], vm[1]); scale2(sm[2], sm[3], vm[2], vm[3]);
            mx = fmaxf(mx, fmaxf(fmaxf(vm[0], vm[1]), fmaxf(vm[2], vm[3])));
            auto across = [](float x, auto op) {
                const unsigned u = __builtin_bit_cast(unsigned, x);
                const auto p = __builtin_amdgcn_permlane16_swap(u, u, false, false);
                const unsigned p0 = p[0], p1 = p[1];
                const unsigned y = __builtin_bit_cast(unsigned, op(__builtin_bit_cast(float, p0), __builtin_bit_cast(float, p1)));
                const auto r = __builtin_amdgcn_permlane32_swap(y, y, false, false);
                const unsigned r0 = r[0], r1 = r[1];
                return op(__builtin_bit_cast(float, r0), __builtin_bit_cast(float, r1));
            };
            mx = across(mx, [](float x, float y) { return fmaxf(x, y); });
            float sum = 0.f;
#pragma unroll
            for (int c = 0; c < 4; ++c) {
                v0[c] = __builtin_amdgcn_exp2f((v0[c] - mx) * l2e); v1[c] = __builtin_amdgcn_exp2f((v1[c] - mx) * l2e); vm[c] = __builtin_amdgcn_exp2f((vm[c] - mx) * l2e);
                sum += v0[c] + v1[c] + 0.5f * vm[c];
            }
            const float rs = __builtin_amdgcn_rcpf(across(sum, [](float x, float y) { return x + y; }));
            st128c((v4u){pk(v0[0] * rs, v1[0] * rs), pk(v0[1] * rs, v1[1] * rs), pk(v0[2] * rs, v1[2] * rs), pk(v0[3] * rs, v1[3] * rs)}, rpx, lane * 16, ((s * NH + h) * 2 + tt) * 1024);
            st64c((v2u){pk(vm[0] * rs, vm[1] * rs), pk(vm[2] * rs, vm[3] * rs)}, rpm, g < 2 ? n * 16 + g * 8 : OOB, ((s * 2 + tt) * NH + h) * 256);
            if (it >= 31) { DRAIN(); st32c(it - 3, rc, lane == 0 ? C_PTF + id * 128 + (wave & 1) * 4 : OOB, 0); }
            DGT(14 + it);
        }
        if (it < 32) BAR();
    }
    DRAIN();
}

__device__ __forceinline__ void h_role(const Args& a, LAS unsigned char* L, int id, int wave, int lane DGARG) {
    const int cg = id >> 1, cls = id & 1, g = lane >> 4, n = lane & 15;
    const rsrc_t rc = mkrsrc(a.ws + O_COMM, COMM_BYTES), rx = mkrsrc(a.ws + O_XN, 2u << 20), rh = mkrsrc(a.ws + O_HH, 7u << 20);
    v4u xs[8];
    auto x_load = [&](int j) {
#pragma unroll
        for (int q = 0; q < 8; ++q) xs[q] = ld128c(rx, lane * 16 + (q & 3) * 1024, (2 * j + cls) * 32768 + wave * 8192 + (q >> 2) * 4096);
    };
    auto x_put = [&](int j) {
#pragma unroll
        for (int q = 0; q < 8; ++q) *(LAS v4u*)(L + (j & 1) * 32768 + wave * 8192 + q * 1024 + lane * 16) = xs[q];
    };
    nap(a.dq);
    if (!wait_xn(rc, 0, lane, false)) return;
    x_load(0);
    v4u W[16][4];
    load_w4<16, 7, true>(mkrsrc(a.w1, DM * FF * 2), (unsigned)(8 * g * FF + 256 * cg + 64 * wave + 4 * n) * 2, FF * 2, NatRow{0}, W);
    x_put(0);
    x_load(1);
    BAR();
    DGT(1);
    for (int it = 0; it < 32; ++it) {
        if (it == 6) wait_xn(rc, 0, lane, true);
        f4 acc[4][2];
        LAS unsigned char* xd = L + ((it + 1) & 1) * 32768 + wave * 8192 + lane * 16;
        const unsigned xso = (2 * min(it + 2, 31) + cls) * 32768 + wave * 8192;
        proj_seg(W, L + (it & 1) * 32768, lane, 0, acc, [&](int k, int half) {
            if (k < 8) { if (half == 0) *(LAS v4u*)(xd + k * 1024) = xs[k]; else xs[k] = ld128c(rx, lane * 16 + (k & 3) * 1024, xso + (k >> 2) * 4096); }
            if (k == 15 && half == 1) DRAIN();
        });
#pragma unroll
        for (int t = 0; t < 4; ++t)
#pragma unroll
            for (int tt = 0; tt < 2; ++tt)
#pragma unroll
                for (int c = 0; c < 4; ++c) { const float v = rbf(acc[t][tt][c]); acc[t][tt][c] = v * __builtin_amdgcn_rcpf(1.f + __builtin_amdgcn_exp2f(v * -1.4426950408889634f)); }
#pragma unroll
        for (int tt = 0; tt < 2; ++tt)
#pragma unroll
            for (int y = 0; y < 2; ++y) st128c(pack2(acc[2 * y][tt], acc[2 * y + 1][tt]), rh, lane * 16 + y * 1024, (((2 * it + cls) * 2 + tt) * 56 + 8 * cg + 2 * wave) * 1024);
        if (it == 31) DRAIN();
        BAR();
        if (wave == 0) st32c(it == 31 ? 32 : it, rc, lane == 0 ? C_HF + id * 128 : OOB, 0);
        DGT(64 + it);
    }
    DRAIN();
}

__device__ __forceinline__ void vo_role(const Args& a, LAS unsigned char* L, int id, bool helper, int wave, int lane DGARG) {
    const int ni = helper ? NHELP : NSEG - NHELP;
    auto seg = [&](int j) { return min(2 * j + (int)helper, j + (helper ? NSEG : NHELP)); };
    const int h = id, g = lane >> 4, n = lane & 15;
    const rsrc_t rc = mkrsrc(a.ws + O_COMM, COMM_BYTES);
    if (wave < 2) {
        const rsrc_t rx = mkrsrc(a.ws + O_XN, 2u << 20);
        constexpr int XO = 16384;
        v4u xs[16];
        v4u W[16][4];
        load_w4<16, 7, true>(mkrsrc(a.wv, DM * 4096 * 2), (unsigned)(8 * g * 4096 + h * HD + 64 * wave + 4 * n) * 2, 8192, NatRow{0}, W);
        BAR();
#pragma unroll
        for (int q = 0; q < 16; ++q) xs[q] = ld128c(rx, lane * 16, seg(2) * 32768 + wave * 16384 + q * 1024);
        DGT(1);
        f4 accA[4][2] = {}, accB[4][2] = {};
        LAS unsigned char* vd = L + wave * 2048 + lane * 16;
        auto put = [&](f4 (&pv)[4][2], int q, int slot) { *(LAS v4u*)(vd + slot * 8192 + (q >> 1) * 4096 + (q & 1) * 1024) = pack2(pv[2 * (q & 1)][q >> 1], pv[2 * (q & 1) + 1][q >> 1]); };
        LAS unsigned char* b0 = L + XO + lane * 16; LAS unsigned char* b1 = b0 + 32768; LAS unsigned char* b2 = b0 + 65536;
        v4u xf[4][2];
#pragma unroll
        for (int k = 0; k < 2; ++k) { xf[k][0] = *(const LAS v4u*)(b0 + k * 1024); xf[k][1] = *(const LAS v4u*)(b0 + 16384 + k * 1024); }
        auto item = [&](int it, f4 (&acc)[4][2], f4 (&pv)[4][2], int pslot) {
            const unsigned xso = seg(min(it + 3, ni - 1)) * 32768 + wave * 16384;
            LAS unsigned char* xw = b2 + wave * 16384;
#pragma unroll
            for (int k = 0; k < 16; ++k) {
#pragma unroll
                for (int t = 0; t < 4; ++t) {
                    SCHED();
                    if (k == 0) { MMA_WA0(acc[t][0], W[k][t], xf[k & 3][0]); MMA_WA0(acc[t][1], W[k][t], xf[k & 3][1]); }
                    else { MMA_WA(acc[t][0], W[k][t], xf[k & 3][0]); MMA_WA(acc[t][1], W[k][t], xf[k & 3][1]); }
                    SCHED();
                    if (t == 0) {
                        const LAS unsigned char* src = (k + 2 < 16 ? b0 : b1) + ((k + 2) & 15) * 1024;
                        xf[(k + 2) & 3][0] = *(const LAS v4u*)src; xf[(k + 2) & 3][1] = *(const LAS v4u*)(src + 16384);
                    }
                    if (k == 1 && t == 0) asm volatile("" : "+v"(pv[0][0]), "+v"(pv[0][1]), "+v"(pv[1][0]), "+v"(pv[1][1]), "+v"(pv[2][0]), "+v"(pv[2][1]), "+v"(pv[3][0]), "+v"(pv[3][1]));
                    if (k >= 1 && k <= 2 && (t & 1)) put(pv, 2 * (k - 1) + (t >> 1), pslot);
                    if (k == 3 && t == 3) BAR();
                    if (k >= 4 && t >= 1) {
                        const int e = 3 * (k - 4) + t - 1, q = e >> 1;
                        if (q < 16) { if (!(e & 1)) *(LAS v4u*)(xw + q * 1024) = xs[q]; else xs[q] = ld128c(rx, lane * 16, xso + q * 1024); }
                    }
                }
            }
            SCHED();
            LAS unsigned char* tb = b0; b0 = b1; b1 = b2; b2 = tb;
        };
        for (int it = 0; it < ni; it += 2) {
            if (it == 12) wait_xn(rc, 4 + (id >> 3), lane, true);
            item(it, accA, accB, 1); item(it + 1, accB, accA, 0);
        }
        asm volatile("s_nop 7" : "+v"(accB[0][0]), "+v"(accB[0][1]), "+v"(accB[1][0]), "+v"(accB[1][1]), "+v"(accB[2][0]), "+v"(accB[2][1]), "+v"(accB[3][0]), "+v"(accB[3][1]));
#pragma unroll
        for (int q = 0; q < 4; ++q) put(accB, q, 1);
        BAR();
        BAR();
        BAR();
    } else {
        const rsrc_t ro = mkrsrc(a.wo, 4096 * DM * 2), rv = mkrsrc(a.ws + O_VO, 64u << 20);
        v4u W[8][8];
        const unsigned wv = (unsigned)((h * HD + 16 * g) * DM + 256 * (wave & 1) + 8 * n) * 2;
        load_w8<8, 5, true>(ro, wv, 256, [&](int k, int jj) { return PairRow()(k & 3, jj) * 4 + (k >> 2); }, W);
        {
            const rsrc_t rx = mkrsrc(a.ws + O_XN, 2u << 20);
            wait_init(rc, init_ld(rc));
            wait_xn(rc, 4 + (id >> 3), lane, false);
            v4u xs[32];
#pragma unroll
            for (int q = 0; q < 32; ++q) xs[q] = ld128c(rx, lane * 16, seg(wave & 1) * 32768 + q * 1024);
#pragma unroll
            for (int q = 0; q < 32; ++q) *(LAS v4u*)(L + 16384 + (wave & 1) * 32768 + q * 1024 + lane * 16) = xs[q];
        }
        const unsigned sv = (unsigned)(32 * (wave & 1) + 8 * (n >> 3)) * 16384u + h * 512 + g * 128 + (n & 7) * 16;
        const unsigned fv = (wave == 2 && lane < (NHELP ? 1 : 2)) ? C_VOF + id * 128 + (helper ? 4 : lane * 4) : OOB;
        v4u va[2][4], vb[2][4];
        auto v_read = [&](v4u (&v)[2][4], int slot) {
#pragma unroll
            for (int q = 0; q < 8; ++q) v[q >> 2][q & 3] = *(const LAS v4u*)(L + slot * 8192 + q * 1024 + lane * 16);
        };
        f4 dl[2][2] = {};
        unsigned svl = OOB, sol = 0;
        BAR();
        BAR();
        BAR();
        v_read(va, 0);
        const unsigned cs = ck();
        auto item = [&](int j, v4u (&v)[2][4], v4u (&vn)[2][4], int nslot) {
            const unsigned so = (unsigned)seg(j) * 1048576u;
            f4 d[16][2];
            v4u o[2];
#pragma unroll
            for (int tp = 0; tp < 8; ++tp) {
                const int t = 2 * tp, u = t - 2;
#pragma unroll
                for (int m = 0; m < 16; ++m) {
                    const int ks = m >> 2, tq = t + ((m >> 1) & 1), r = m & 1;
                    SCHED();
                    if (ks == 0) MMA_WB0(d[tq][r], v[r][0], W[(tq >> 3) * 4][tq & 7]); else MMA_WB(d[tq][r], v[r][ks], W[(tq >> 3) * 4 + ks][tq & 7]);
                    SCHED();
                    if (tp == 6 && m == 1) {
                        if (__builtin_expect(j < 14, 0)) asm volatile("s_waitcnt vmcnt(10)" ::: "memory"); else asm volatile("s_waitcnt vmcnt(26)" ::: "memory");
                        BAR();
                        v_read(vn, nslot);
                        st32c(seg(j < 14 ? j : j - 1), rc, fv, 0);
                    }
                    if (m >= 6) {
                        const int k = m - 6, w = k / 5, e = k % 5;
                        f4& p0 = tp ? d[u + w][0] : dl[w][0]; f4& p1 = tp ? d[u + w][1] : dl[w][1];
                        if (e == 0) asm volatile("" : "+v"(p0), "+v"(p1));
                        if (e < 4) o[w][e] = pk(p0[e], p1[e]);
                        else if (tp) st128r(o[w], rv, sv, so + (((u + w) >> 3) * 16 + ((u + w) & 7)) * 16384u);
                        else st128r(o[w], rv, svl, sol + (22 + w) * 16384u);
                    }
                }
            }
            dl[0][0] = d[14][0]; dl[0][1] = d[14][1]; dl[1][0] = d[15][0]; dl[1][1] = d[15][1];
            svl = sv; sol = so;
            if (j < 2) DGT(64 + j);
        };
        for (int j = 0; j < ni; j += 2) { item(j, va, vb, 1); item(j + 1, vb, va, 0); }
        asm volatile("s_nop 7" : "+v"(dl[0][0]), "+v"(dl[0][1]), "+v"(dl[1][0]), "+v"(dl[1][1]));
        st128c(pack2(dl[0][0], dl[0][1]), rv, sv, sol + 22 * 16384u); st128c(pack2(dl[1][0], dl[1][1]), rv, sv, sol + 23 * 16384u);
        DRAIN();
        BAR();
        st32c(NSEG, rc, fv, 0);
        DGT(127); DG(13, ck() - cs);
    }
}

__device__ __forceinline__ void out_role(const Args& a, LAS unsigned char* L, int id, int wave, int lane DGARG) {
    const int cgp = id & 7, r8 = id >> 3, g = lane >> 4, n = lane & 15;
    const rsrc_t rc = mkrsrc(a.ws + O_COMM, COMM_BYTES), rh = mkrsrc(a.ws + O_HH, 7u << 20), rv = mkrsrc(a.ws + O_VO, 64u << 20), rum = mkrsrc(a.ws + O_UM, 16u << 20),
                 rpx = mkrsrc(a.ws + O_PX, 4u << 20), rpm = mkrsrc(a.ws + O_PM, 1u << 20);
    v4u W[14][4];
    load_w4<14, 7, true>(mkrsrc(a.w2, FF * DM * 2), (unsigned)((448 * wave + 16 * g) * DM + 64 * cgp + 8 * (n & 7) + 4 * (n >> 3)) * 2, DM * 2, PairRow(), W);
    const unsigned uxv = (unsigned)(8 * cgp + 4 * (n >> 3)) * 16384u + wave * 4096 + g * 128 + (n & 7) * 16;
    const unsigned umv = (unsigned)(8 * cgp + 4 * (n >> 3)) * 4096u + wave * 1024 + g * 128 + (n & 7) * 16;
    DGT(8);
    const unsigned fv = lane < 16 ? C_PTF + ((8 * wave + (lane & 7)) * 2 + (r8 & 1)) * 128 + ((lane >> 3) & 1) * 4
                      : lane < 32 ? C_UMF + (8 * cgp + (lane & 7)) * 128
                      : lane < 48 ? C_VOF + (8 * wave + (lane & 7)) * 128 + ((lane >> 3) & 1) * 4
                                  : C_HF + (min(lane - 48, 6) * 2 + (r8 & 1)) * 128;
    auto want = [&](int sl_, int se) -> unsigned { return lane < 16 ? (sl_ >> 1) + 1 : lane < 32 ? sl_ + 1 : lane < 48 ? se + 1 : (se >> 1) + 1; };
    const rsrc_t rxi = mkrsrc(a.x, SEQ * DM * 2), rout = mkrsrc(a.out, SEQ * DM * 2);
    const unsigned ov = ((wave * 8 + (lane >> 3)) * DM + 64 * cgp + 8 * (lane & 7)) * 2;
    v4u hf[14][2];
    auto h_load = [&](int q, unsigned so) { hf[q >> 1][q & 1] = ld128c(rh, lane * 16, so + (q & 1) * 57344 + (q >> 1) * 1024); };
    if (!wait_flags(rc, fv, lane < 32 ? 0 : want(0, r8))) return;
#pragma unroll
    for (int q = 0; q < 16; ++q) h_load(q, (r8 * 112 + 14 * wave) * 1024);
    for (int u = 0; u < 16; ++u) {
        const int s = 4 * u + r8, sn = min(s + 4, NSEG - 4 + r8);
        f4 acc[4][2];
        v4u ux[8][4];
        FENCE();
        const unsigned fl = __builtin_amdgcn_raw_buffer_load_b32(rc, fv, 0, 16);
        v4u xv;
        const unsigned sux = (unsigned)s * 1048576u, shc = __builtin_amdgcn_readfirstlane((s * 112 + 14 * wave) * 1024);
#pragma unroll
        for (int q = 16; q < 28; ++q) h_load(q, shc);
#pragma unroll
        for (int ks = 0; ks < 14; ++ks) {
#pragma unroll
            for (int t = 0; t < 4; ++t) {
                SCHED();
                const int q = 3 * ks + t;
                if (t < 3 && q < 32) ux[7 - (q >> 2)][q & 3] = ld128c(rv, uxv, sux + (q & 3) * 16384u + (7 - (q >> 2)) * 512);
                SCHED();
                if (ks == 0) { MMA_WA0(acc[t][0], W[ks][t], hf[ks][0]); MMA_WA0(acc[t][1], W[ks][t], hf[ks][1]); }
                else { MMA_WA(acc[t][0], W[ks][t], hf[ks][0]); MMA_WA(acc[t][1], W[ks][t], hf[ks][1]); }
            }
        }
        SCHED();
        if (__builtin_amdgcn_ballot_w64(fl < want(s, sn)) != 0) { if (!wait_flags(rc, fv, want(s, sn))) return; }
        FENCE();
        {
            const int s2 = min(sn + 4, NSEG - 4 + r8);
            const unsigned long long nr = __builtin_amdgcn_ballot_w64(fl < want(sn + 2, s2 + 1));
            const bool far = ((nr >> 32) & 0xffff) == 0;
            st32c((far ? s2 : sn) | ((nr >> 48) ? 0 : 256) | ((unsigned)nr ? 0 : 512), rc, (cgp == 0 && wave == 0 && lane == 0) ? C_OUTP + r8 * 128 : OOB, 0);
        }
        DGT(16 + 2 * u);
        {
            v4u px[8][2], pm[2][2], um[2][4];
            const unsigned spx = __builtin_amdgcn_readfirstlane((s * NH + 8 * wave) * 2048), spm = __builtin_amdgcn_readfirstlane((s * 16 + 2 * wave) * 1024);
            const unsigned shn = __builtin_amdgcn_readfirstlane((sn * 112 + 14 * wave) * 1024);
            auto p_load = [&](int hh) {
#pragma unroll
                for (int tt = 0; tt < 2; ++tt) { px[hh][tt] = ld128c(rpx, lane * 16, spx + hh * 2048 + tt * 1024); SCHED(); }
            };
#pragma unroll
            for (int hh = 0; hh < 5; ++hh) p_load(hh);
#pragma unroll
            for (int hh = 0; hh < 8; ++hh) {
#pragma unroll
                for (int t = 0; t < 4; ++t) {
                    if (hh == 7) { MMA_WA(acc[t][0], ux[hh][t], px[hh][0]); MMA_WA(acc[t][1], ux[hh][t], px[hh][1]); }
                    else { MMA_VV(acc[t][0], ux[hh][t], px[hh][0]); MMA_VV(acc[t][1], ux[hh][t], px[hh][1]); }
                }
                SCHED();
                if (hh == 0) { p_load(5); p_load(6); p_load(7); }
                else if (hh < 3) {
#pragma unroll
                    for (int tt = 0; tt < 2; ++tt) pm[hh - 1][tt] = ld128c(rpm, lane * 16, spm + tt * 8192 + (hh - 1) * 1024);
#pragma unroll
                    for (int t = 0; t < 4; ++t) um[hh - 1][t] = ld128c(rum, umv, (unsigned)s * 262144u + t * 4096u + (hh - 1) * 512);
                } else if (hh < 7) {
#pragma unroll
                    for (int q = 4 * (hh - 3); q < 4 * (hh - 2); ++q) h_load(q, shn);
                }
                if (hh == 2) xv = ld128(rxi, ov, s * 32768);
                SCHED();
            }
#pragma unroll
            for (int F = 0; F < 2; ++F)
#pragma unroll
                for (int t = 0; t < 4; ++t) { MMA_VV(acc[t][0], um[F][t], pm[F][0]); MMA_VV(acc[t][1], um[F][t], pm[F][1]); }
        }
        constexpr int RS = 272;
        asm volatile("s_nop 7" : "+v"(acc[0][0]), "+v"(acc[0][1]), "+v"(acc[1][0]), "+v"(acc[1][1]), "+v"(acc[2][0]), "+v"(acc[2][1]), "+v"(acc[3][0]), "+v"(acc[3][1]));
        BAR();
#pragma unroll
        for (int tt = 0; tt < 2; ++tt)
#pragma unroll
            for (int c = 0; c < 4; ++c) {
                const int r = 4 * g + c;
                *(LAS f4*)(L + wave * (32 * RS) + (16 * tt + n) * RS + (8 * (r & 7) + 4 * (r >> 3)) * 4) = (f4){acc[0][tt][c], acc[1][tt][c], acc[2][tt][c], acc[3][tt][c]};
            }
        BAR();
        {
            const int tok = wave * 8 + (lane >> 3), c8 = lane & 7;
            f4 lo = {0, 0, 0, 0}, hi = lo;
#pragma unroll
            for (int w = 0; w < 4; ++w) { lo += *(const LAS f4*)(L + w * (32 * RS) + tok * RS + c8 * 32); hi += *(const LAS f4*)(L + w * (32 * RS) + tok * RS + c8 * 32 + 16); }
            const v4u r = {pk(lo[0] + bflo(xv.x), lo[1] + bfhi(xv.x)), pk(lo[2] + bflo(xv.y), lo[3] + bfhi(xv.y)),
                           pk(hi[0] + bflo(xv.z), hi[1] + bfhi(xv.z)), pk(hi[2] + bflo(xv.w), hi[3] + bfhi(xv.w))};
            __builtin_amdgcn_raw_buffer_store_b128(r, rout, ov, s * 32768, 0);
            asm volatile("s_nop 1" :: "v"(r) : "memory");
        }
        DGT(17 + 2 * u);
    }
}

}

extern "C" __global__ void __launch_bounds__(256, 1) rmt_main(Args a) {
    extern __shared__ __attribute__((aligned(16))) unsigned char lds[];
    LAS unsigned char* L = (LAS unsigned char*)lds;
    const int wave = __builtin_amdgcn_readfirstlane(threadIdx.x >> 6), lane = threadIdx.x & 63;
    if (wave == 1) {
        unsigned long long c0, c1;
        asm volatile("s_getpc_b64 vcc\n\ts_add_u32 vcc_lo, vcc_lo, rmt_main@rel32@lo+4\n\ts_addc_u32 vcc_hi, vcc_hi, rmt_main@rel32@hi+12\n\ts_mov_b64 %0, vcc\n\t"
                     "s_getpc_b64 vcc\n\ts_add_u32 vcc_lo, vcc_lo, rmt_init@rel32@lo+4\n\ts_addc_u32 vcc_hi, vcc_hi, rmt_init@rel32@hi+12\n\ts_mov_b64 %1, vcc"
                     : "=s"(c0), "=s"(c1) :: "vcc", "scc");
        const unsigned long long q = c0 + ((blockIdx.x >> 3) * 64 + lane) * 128;
        const unsigned long long src = q + 4 <= c1 ? q : c0;
        unsigned keep;
        asm volatile("s_mov_b32 %0, m0\n\ts_mov_b32 m0, %2\n\ts_nop 0\n\tglobal_load_lds_dword %1, off\n\ts_mov_b32 m0, %0" : "=&s"(keep) : "v"(src), "s"((unsigned)(uintptr_t)L + 147456u) : "memory");
    }
    const int role = (blockIdx.x & 7) * 32 + (blockIdx.x >> 3);
    const int cls = role >> 5, bi = role & 31;
    if (cls < 4) {
        const int rep = (cls & 1) * 16 + (cls >> 1) * 8 + (bi & 7);
        if (bi < 8) {
            const int head = cls * 8 + bi;
            if (head == 0) reset_words(mkrsrc(a.ws + O_COMM, COMM_BYTES), threadIdx.x);
            if (head == 16) { if (wave == 2) probe_pages(a, lane DGPASS); BAR(); }
            if (wave == 0) head_wave<0>(a, L, head, lane, rep DGPASS); else if (wave == 1) head_wave<1>(a, L, head, lane, rep DGPASS);
            else if (wave == 2) head_wave<2>(a, L, head, lane, rep DGPASS); else head_wave<3>(a, L, head, lane, rep DGPASS);
        } else if (bi < 24) {
            const int sl = cls * 16 + bi - 8;
            v4u xa[2];
            ln_load<1>(a, sl, wave, lane, xa);
            SCHED();
            make_b<1>(a, L, sl >> 1, 2 + (sl & 1), 64 + sl, wave, lane, LnHook<0, 16>{a, L + 65536, sl, wave, lane, xa, {}} DGPASS);
            if (wave == 0) slice_wave<0>(a, L, sl, lane, rep DGPASS); else if (wave == 1) slice_wave<1>(a, L, sl, lane, rep DGPASS);
            else if (wave == 2) slice_wave<2>(a, L, sl, lane, rep DGPASS); else slice_wave<3>(a, L, sl, lane, rep DGPASS);
        } else {
            make_b<2>(a, L, cls * 8 + bi - 24, 0, (cls * 8 + bi - 24) * 2, wave, lane, NoHook() DGPASS);
            if (bi < 26 || bi == 30 || bi == (cls < 2 ? 31 : 29)) ln_rest(a, L, cls * 4 + (bi < 26 ? bi - 24 : bi == 30 ? 2 : 3), wave, lane);
            if (bi < 26 || bi == 30) {
                prefetcher(a, cls, (bi == 30 ? 2 : bi - 24) + 3 * wave, 12, lane);
            } else if (bi < 30) {
                const int id = (bi - 26) * 4 + cls;
                if (id < 14) h_role(a, L, id, wave, lane DGPASS);
            } else if (bi == 31 && cls == 2) {
                open_rings(a, threadIdx.x, lane);
                if (wave == 0) metronome(a, lane DGPASS);
            } else if (bi == 31 && cls == 3 && wave == 0) {
                gatekeeper(a, lane);
            }
        }
    } else {
        if (bi < 16) kq_role(a, L, (cls - 4) * 16 + bi, wave, lane DGPASS);
        else if (bi < 24) vo_role(a, L, (cls - 4) * 8 + (bi & 7), false, wave, lane DGPASS);
        else {
            const int id = (cls - 4) * 8 + bi - 24;
            make_b<4>(a, L, id, 4, id * 2 + 1, wave, lane, NoHook() DGPASS);
            out_role(a, L, id, wave, lane DGPASS);
        }
    }
}

extern "C" __global__ void rmt_init() {}

constexpr int g_tune[13] = {T_PERIOD, T_SEND, T_DA, T_DB, T_GUARD, T_DU, T_SENDB, T_DMS, T_DK, T_DQ, T_GH, T_GS, T_DECAY};
extern "C" void kernel_launch(void* const* d_in, const int* in_sizes, int n_in, void* d_out, int out_size, void* d_ws, size_t ws_size, hipStream_t stream) {
    Args a;
    a.x = (const u16*)d_in[0]; a.mem0 = (const u16*)d_in[1]; a.wq = (const u16*)d_in[2]; a.wk = (const u16*)d_in[3]; a.wv = (const u16*)d_in[4]; a.wo = (const u16*)d_in[5];
    a.w1 = (const u16*)d_in[8]; a.w2 = (const u16*)d_in[9];
    a.out = (u16*)d_out;
    a.ws = (unsigned char*)(((uintptr_t)d_ws + (512u << 10) - 1) & ~(uintptr_t)((512u << 10) - 1));
    a.period = g_tune[0]; a.send = g_tune[1]; a.da = g_tune[2]; a.db = g_tune[3]; a.guard = g_tune[4]; a.du = g_tune[5]; a.sendb = g_tune[6]; a.dms = g_tune[7]; a.dk = g_tune[8]; a.dq = g_tune[9]; a.gh = g_tune[10]; a.gs = g_tune[11]; a.decay = g_tune[12]; a.pad = 0;
    constexpr int kLds = 144 * 1024 + 256;
    (void)hipFuncSetAttribute((const void*)rmt_main, hipFuncAttributeMaxDynamicSharedMemorySize, kLds);
    rmt_main<<<256, 256, kLds, stream>>>(a);
}
```
